```python
import torch

def _softmax(x, axis=-1):
    return torch.softmax(x, dim=axis)
def _gelu(x):
    return torch.nn.functional.gelu(x, approximate="tanh")
def _silu(x):
    return torch.nn.functional.silu(x)
def _sigmoid(x):
    return torch.sigmoid(x)
def _layernorm(x, axis=-1, eps=1e-5):
    m = x.mean(dim=axis, keepdim=True)
    v = x.var(dim=axis, keepdim=True, unbiased=False)
    return (x - m) / torch.sqrt(v + eps)
def _scatter_add(tgt, idx, src, axis=0):
    return tgt.clone().index_add_(axis, idx, src)
def _segment_sum(src, seg, n):
    out = torch.zeros((n,) + src.shape[1:], dtype=src.dtype)
    return out.index_add_(0, seg, src)
def _pad(x, pw):
    flat = tuple(v for pair in reversed(pw) for v in pair)
    return torch.nn.functional.pad(x, flat)

kernel_name = 'sequential_preLN__softmax_full_mha__mixer_s_mlp__prompt'

def setup_inputs():

    _w = (lambda a: (lambda t: t.to(torch.bfloat16) if t.is_floating_point() else t)(torch.as_tensor(a)))
    def _A_setup():
        B, S, D = 1, 2048, 512
        return {
            "x": _w((lambda *s: torch.randn(*s, dtype=torch.bfloat16) * 0.05)(B, S, D)),
            "wqkv": _w((lambda *s: torch.randn(*s, dtype=torch.bfloat16) * 0.05)(D, 3 * D)),
            "wo": _w((lambda *s: torch.randn(*s, dtype=torch.bfloat16) * 0.05)(D, D)),
        }
    _A_in = _A_setup()

    def _B_setup():
        B, S, D, F = 1, 2048, 512, 1376
        return {
            "x":  _w((lambda *s: torch.randn(*s, dtype=torch.bfloat16) * 0.05)(B, S, D)),
            "w1": _w((lambda *s: torch.randn(*s, dtype=torch.bfloat16) * 3.2)(D, D) * (D ** -0.5)),
            "w2": _w((lambda *s: torch.randn(*s, dtype=torch.bfloat16) * 3.2)(D, D) * (D ** -0.5)),
            "w3": _w((lambda *s: torch.randn(*s, dtype=torch.bfloat16) * 3.2)(D, F) * (D ** -0.5)),
            "w4": _w((lambda *s: torch.randn(*s, dtype=torch.bfloat16) * 3.2)(F, D) * (F ** -0.5)),
        }
    _B_in = _B_setup()

    return {
        "A_x": _A_in["x"],
        "A_wqkv": _A_in["wqkv"],
        "A_wo": _A_in["wo"],
        "B_x": _B_in["x"],
        "B_w1": _B_in["w1"],
        "B_w2": _B_in["w2"],
        "B_w3": _B_in["w3"],
        "B_w4": _B_in["w4"],
    }

def reference(A_x, A_wqkv, A_wo, B_x, B_w1, B_w2, B_w3, B_w4):
    def _A_ref(x, wqkv, wo):
        B, S, D = x.shape
        H = 32; Dh = D // H
        qkv = (lambda x, *d: x.permute(*d))((x @ wqkv).reshape(B, S, 3, H, Dh), 2, 0, 3, 1, 4)
        q, k, v = qkv[0], qkv[1], qkv[2]
        scale = Dh ** -0.5
        scores = torch.einsum("bhqd,bhkd->bhqk", q, k) * scale
        p = _softmax(scores, axis=-1)
        o = torch.einsum("bhqk,bhkd->bhqd", p, v)
        return (lambda x, *d: x.permute(*d))(o, 0, 2, 1, 3).reshape(B, S, D) @ wo

    def _B_ref(x, w1, w2, w3, w4):
        B, S, D = x.shape
        h = _gelu(_layernorm(x, axis=-1) @ w1)
        c = D // 4
        g0 = _pad(h[:, :, 0*c:1*c], ((0, 0), (1, 0), (0, 0)))[:, :S, :]
        g1 = _pad(h[:, :, 1*c:2*c], ((0, 0), (0, 1), (0, 0)))[:, 1:, :]
        g2 = _pad(h[:, :, 2*c:3*c], ((0, 0), (2, 0), (0, 0)))[:, :S, :]
        g3 = _pad(h[:, :, 3*c:   ], ((0, 0), (0, 2), (0, 0)))[:, 2:, :]
        hs = torch.concatenate([g0, g1, g2, g3], axis=-1) @ w2
        x1 = x + hs
        h2 = _gelu(_layernorm(x1, axis=-1) @ w3) @ w4
        return x1 + h2

    x = A_x
    h = x + _A_ref(_layernorm(x, axis=-1), A_wqkv, A_wo)
    h2 = h + _B_ref(_layernorm(h, axis=-1), B_w1, B_w2, B_w3, B_w4)
    return h2
```

```cpp
#include <hip/hip_runtime.h>
#include <hip/hip_bf16.h>
#include <stdint.h>

typedef unsigned short u16;
typedef __attribute__((ext_vector_type(8))) short bf16x8;
typedef __attribute__((ext_vector_type(4))) short s16x4;
typedef __attribute__((ext_vector_type(2))) float f32x2;
typedef __attribute__((ext_vector_type(4))) float f32x4;
typedef __attribute__((ext_vector_type(16))) float f32x16;
typedef __attribute__((ext_vector_type(2))) __bf16 bf16x2_t;
typedef __attribute__((ext_vector_type(4))) unsigned u32x4;
typedef __attribute__((ext_vector_type(2))) unsigned u32x2;
typedef short v4i16_t __attribute__((ext_vector_type(4)));
typedef __attribute__((address_space(3))) const char* lds_cptr;

#define DEV __device__ __forceinline__
#define MFMA16(a, b, c) __builtin_amdgcn_mfma_f32_16x16x32_bf16((a), (b), (c), 0, 0, 0)
#define MFMA32(a, b, c) __builtin_amdgcn_mfma_f32_32x32x16_bf16((a), (b), (c), 0, 0, 0)

constexpr int SEQ = 2048, DM = 512, NH = 32, FF = 1376;

constexpr size_t WS_Q = 0, WS_K = 2u << 20, WS_V = 4u << 20, WS_O = 6u << 20, WS_H = 8u << 20, WS_Y = 10u << 20,
                 WS_T = 12u << 20, WS_X1 = 14u << 20, WS_U = 16u << 20;

DEV unsigned cvtpk(float lo, float hi) {
  f32x2 v = {lo, hi};
  bf16x2_t b = __builtin_convertvector(v, bf16x2_t);
  return __builtin_bit_cast(unsigned, b);
}
DEV float lo2f(unsigned p) { return __uint_as_float(p << 16); }
DEV float hi2f(unsigned p) { return __uint_as_float(p & 0xffff0000u); }
DEV float rbf(float x) { return lo2f(cvtpk(x, x)); }

DEV void glds16(const void* gbase, unsigned voff, unsigned lds_base) {
  unsigned sv;
  asm volatile("s_mov_b32 %0, m0\n\ts_mov_b32 m0, %3\n\ts_nop 0\n\tglobal_load_lds_dwordx4 %1, %2\n\ts_mov_b32 m0, %0"
               : "=&s"(sv)
               : "v"(voff), "s"(gbase), "s"(lds_base)
               : "memory");
}
template <int N>
DEV void wait_vm_bar() {
  asm volatile("s_waitcnt vmcnt(%0) lgkmcnt(0)\n\ts_barrier" ::"i"(N) : "memory");
}
DEV bf16x8 lds_rd128(lds_cptr p) { return *(const __attribute__((address_space(3))) bf16x8*)p; }
DEV s16x4 lds_tr(lds_cptr p) {
  return __builtin_bit_cast(s16x4, __builtin_amdgcn_ds_read_tr16_b64_v4i16((__attribute__((address_space(3))) v4i16_t*)p));
}
template <int CTRL>
DEV float dpp_add(float v) {
  int x = __builtin_amdgcn_update_dpp(0, __float_as_int(v), CTRL, 0xf, 0xf, true);
  return v + __int_as_float(x);
}
DEV float wave_sum(float v) {
  v = dpp_add<0x121>(v);
  v = dpp_add<0x122>(v);
  v = dpp_add<0x124>(v);
  v = dpp_add<0x128>(v);
  v += __shfl_xor(v, 16);
  v += __shfl_xor(v, 32);
  return v;
}
DEV float gelu_tanh(float x) {
  const float c = -2.0f * 0.7978845608028654f * 1.4426950408889634f;
  float u = x * (1.0f + 0.044715f * x * x);
  float e = __builtin_amdgcn_exp2f(c * u);
  return x * __builtin_amdgcn_rcpf(1.0f + e);
}
DEV int bswz(int k) { return ((k >> 1) & 1) | (((k >> 3) & 1) << 1); }

template <int I, int N, class F>
DEV void static_for(F&& f) {
  if constexpr (I < N) {
    f(std::integral_constant<int, I>{});
    static_for<I + 1, N>(f);
  }
}

template <int MODE>
struct Cfg {
  static constexpr int NT = (MODE == 1 || MODE == 6) ? 3 : 1;
  static constexpr bool LN = (MODE == 1 || MODE == 4 || MODE == 6);
  static constexpr int K = (MODE == 7) ? FF : DM;
  static constexpr int LDA = K;
  static constexpr int LDW = (MODE == 1) ? 3 * DM : (MODE == 6 ? FF : DM);
  static constexpr int NSTAGE = (K + 63) / 64;
  static constexpr int RS = 128 * NT;
  static constexpr int BBYTES = 64 * RS;
  static constexpr int SLOT = BBYTES + (LN ? 0 : 8192);
  static constexpr int NSLOT = (MODE == 1 || MODE == 6) ? 3 : (MODE == 7 ? 5 : 9);
  static constexpr int RING0 = LN ? 65536 : 0;
  static constexpr int LDS = RING0 + (NSLOT > NSTAGE ? NSTAGE : NSLOT) * SLOT;
  static constexpr int LPS = NT + (LN ? 0 : 1);
};

struct GArgs {
  const u16* A;
  const u16* W;
  const u16* r0;
  const u16* r1;
  u16* o0;
  u16* o1;
  u16* o2;
};

template <int MODE>
__global__ __launch_bounds__(512, 2) void gemm_kernel(GArgs ga) {
  using C = Cfg<MODE>;
  constexpr int NT = C::NT;
  extern __shared__ __attribute__((aligned(16))) char lds[];
  const int tid = threadIdx.x, lane = tid & 63;
  const int wid = __builtin_amdgcn_readfirstlane(tid >> 6);
  const int wm = wid & 1, wn = wid >> 1;
  const int xcd = blockIdx.x & 7, idx = blockIdx.x >> 3;
  const int rb = xcd * 4 + (idx >> 3), cb = idx & 7;
  const int row0 = rb * 64;
  const unsigned lds0 = (unsigned)(uintptr_t)lds;
  const int l15 = lane & 15, g = lane >> 4;

  unsigned boff[NT];
  int bk[NT];
#pragma unroll
  for (int j = 0; j < NT; ++j) {
    int o = (wid * NT + j) * 1024 + lane * 16;
    int k = o / C::RS, rem = o % C::RS;
    int grp = rem >> 7, blk = (rem >> 5) & 3, hf = (rem >> 4) & 1;
    int col;
    if (MODE == 1) col = grp * 512 + cb * 64;
    else col = cb * (64 * NT) + grp * 64;
    col += ((blk ^ bswz(k)) * 16) + hf * 8;
    if (MODE == 6 && col > FF - 8) col = FF - 8;
    bk[j] = k;
    boff[j] = (unsigned)(k * C::LDW + col) * 2u;
  }
  unsigned aoff = 0;
  if (!C::LN) {
    int r = wid * 8 + (lane >> 3), c = (lane & 7) ^ ((r >> 1) & 7);
    aoff = (unsigned)((row0 + r) * C::LDA + c * 8) * 2u;
  }
  auto issue = [&](int s) {
    const unsigned slot = lds0 + C::RING0 + (unsigned)((s % C::NSLOT) * C::SLOT);
    if (!C::LN) glds16(ga.A, aoff + s * 128, slot + C::BBYTES + wid * 1024);
#pragma unroll
    for (int j = 0; j < NT; ++j) {
      unsigned off = boff[j] + (unsigned)(s * 64 * C::LDW) * 2u;
      if (MODE == 7 && s == C::NSTAGE - 1 && s * 64 + bk[j] >= FF) off = boff[j] + (unsigned)((FF - 64) * C::LDW) * 2u;
      glds16(ga.W, off, slot + (unsigned)(wid * NT + j) * 1024u);
    }
  };

  if constexpr (C::LN) {
    u32x4 rv[8];
#pragma unroll
    for (int i = 0; i < 8; ++i) rv[i] = *(const u32x4*)(ga.A + (size_t)(row0 + wid * 8 + i) * DM + lane * 8);
    static_for<0, C::NSLOT - 1>([&](auto s_) { issue(s_.value); });
#pragma unroll
    for (int i = 0; i < 8; ++i) {
      float x[8];
#pragma unroll
      for (int e = 0; e < 4; ++e) { x[2 * e] = lo2f(rv[i][e]); x[2 * e + 1] = hi2f(rv[i][e]); }
      const int NLN = (MODE == 4) ? 2 : 1;
#pragma unroll
      for (int p = 0; p < NLN; ++p) {
        float s = 0.f;
#pragma unroll
        for (int e = 0; e < 8; ++e) s += x[e];
        float m = wave_sum(s) * (1.0f / DM);
        float q = 0.f;
#pragma unroll
        for (int e = 0; e < 8; ++e) { float d = x[e] - m; q += d * d; }
        float v = wave_sum(q) * (1.0f / DM);
        float mb = rbf(m);
        float sd = rbf(__builtin_sqrtf(rbf(rbf(v) + 1e-5f)));
        float rs = 1.0f / sd;
#pragma unroll
        for (int e = 0; e < 8; e += 2) {
          unsigned d = cvtpk(x[e] - mb, x[e + 1] - mb);
          unsigned y = cvtpk(lo2f(d) * rs, hi2f(d) * rs);
          x[e] = lo2f(y); x[e + 1] = hi2f(y);
          rv[i][e >> 1] = y;
        }
        if (MODE == 4 && p == 0) {
          if ((lane >> 3) == cb) *(u32x4*)(ga.o1 + (size_t)(row0 + wid * 8 + i) * DM + lane * 8) = rv[i];
        }
      }
      int r = wid * 8 + i;
      *(u32x4*)(lds + (lane >> 3) * 8192 + r * 128 + (((lane & 7) ^ ((r >> 1) & 7)) * 16)) = rv[i];
    }
  } else {
    static_for<0, (C::NSLOT - 1 < C::NSTAGE ? C::NSLOT - 1 : C::NSTAGE)>([&](auto s_) { issue(s_.value); });
  }

  int xoff[2][2];
#pragma unroll
  for (int mt = 0; mt < 2; ++mt)
#pragma unroll
    for (int kk = 0; kk < 2; ++kk) {
      int r = wm * 32 + mt * 16 + l15;
      xoff[mt][kk] = r * 128 + (((4 * kk + g) ^ ((r >> 1) & 7)) * 16);
    }
  int woff[2][2];
  {
    int q = l15 >> 2, p = l15 & 3;
#pragma unroll
    for (int kk = 0; kk < 2; ++kk)
#pragma unroll
      for (int t = 0; t < 2; ++t) {
        int k = 32 * kk + 8 * g + 4 * t + q;
        woff[kk][t] = k * C::RS + ((wn ^ bswz(k)) * 32) + p * 8;
      }
  }

  f32x4 acc[2][NT];
#pragma unroll
  for (int mt = 0; mt < 2; ++mt)
#pragma unroll
    for (int nt = 0; nt < NT; ++nt) acc[mt][nt] = f32x4{0.f, 0.f, 0.f, 0.f};

  static_for<0, C::NSTAGE>([&](auto s_) {
    constexpr int s = s_.value;
    constexpr int last_issued = (s + C::NSLOT - 2 < C::NSTAGE - 1) ? (s + C::NSLOT - 2) : (C::NSTAGE - 1);
    wait_vm_bar<(last_issued - s) * C::LPS>();
    if constexpr (s + C::NSLOT - 1 < C::NSTAGE) issue(s + C::NSLOT - 1);
    const lds_cptr bs = (lds_cptr)lds + C::RING0 + (s % C::NSLOT) * C::SLOT;
    const lds_cptr as = C::LN ? ((lds_cptr)lds + s * 8192) : (bs + C::BBYTES);
    constexpr int NKK = (MODE == 7 && s == C::NSTAGE - 1) ? 1 : 2;
#pragma unroll
    for (int kk = 0; kk < NKK; ++kk) {
      bf16x8 xf[2];
#pragma unroll
      for (int mt = 0; mt < 2; ++mt) xf[mt] = lds_rd128(as + xoff[mt][kk]);
#pragma unroll
      for (int nt = 0; nt < NT; ++nt) {
        s16x4 w0 = lds_tr(bs + woff[kk][0] + nt * 128);
        s16x4 w1 = lds_tr(bs + woff[kk][1] + nt * 128);
        bf16x8 wf = __builtin_shufflevector(w0, w1, 0, 1, 2, 3, 4, 5, 6, 7);
#pragma unroll
        for (int mt = 0; mt < 2; ++mt) {
          if (MODE == 1 && nt == 2) acc[mt][nt] = MFMA16(xf[mt], wf, acc[mt][nt]);
          else acc[mt][nt] = MFMA16(wf, xf[mt], acc[mt][nt]);
        }
      }
    }
  });

#pragma unroll
  for (int mt = 0; mt < 2; ++mt) {
    const int tok = row0 + wm * 32 + mt * 16 + l15;
#pragma unroll
    for (int nt = 0; nt < NT; ++nt) {
      f32x4 a = acc[mt][nt];
      if constexpr (MODE == 1) {
        const int head = cb * 4 + wn;
        if (nt < 2) {
          float sc = (nt == 0) ? 0.25f * 1.4426950408889634f : 1.0f;
          u32x2 v = {cvtpk(a[0] * sc, a[1] * sc), cvtpk(a[2] * sc, a[3] * sc)};
          u16* dst = (nt == 0 ? ga.o0 : ga.o1) + ((size_t)(head * 64 + (tok >> 5)) * 2 + (g >> 1)) * 256 + (tok & 31) * 8 + (g & 1) * 4;
          *(u32x2*)dst = v;
        } else {
          const int step = (row0 + wm * 32 + mt * 16) >> 4;
          u32x2 v = {cvtpk(a[0], a[1]), cvtpk(a[2], a[3])};
          u16* dst = ga.o2 + ((size_t)(head * 128 + step) * 2 + (g & 1)) * 128 + l15 * 8 + (g >> 1) * 4;
          *(u32x2*)dst = v;
        }
      } else if constexpr (MODE == 3 || MODE == 5) {
        const int col = cb * 64 + wn * 16 + 4 * g;
        u32x2 r = *(const u32x2*)(ga.r0 + (size_t)tok * DM + col);
        unsigned a0 = cvtpk(a[0], a[1]), a1 = cvtpk(a[2], a[3]);
        u32x2 v = {cvtpk(lo2f(r[0]) + lo2f(a0), hi2f(r[0]) + hi2f(a0)), cvtpk(lo2f(r[1]) + lo2f(a1), hi2f(r[1]) + hi2f(a1))};
        *(u32x2*)(ga.o0 + (size_t)tok * DM + col) = v;
      } else if constexpr (MODE == 4) {
        const int col = cb * 64 + wn * 16 + 4 * g;
        unsigned a0 = cvtpk(a[0], a[1]), a1 = cvtpk(a[2], a[3]);
        u32x2 v = {cvtpk(gelu_tanh(lo2f(a0)), gelu_tanh(hi2f(a0))), cvtpk(gelu_tanh(lo2f(a1)), gelu_tanh(hi2f(a1)))};
        const int grp = cb >> 1;
        const int sh = (grp == 0) ? 1 : (grp == 1) ? -1 : (grp == 2) ? 2 : -2;
        const int dt = tok + sh;
        if (dt >= 0 && dt < SEQ) *(u32x2*)(ga.o0 + (size_t)dt * DM + col) = v;
        const int zt = (sh > 0) ? tok : tok;
        const int src = zt - sh;
        if (src < 0 || src >= SEQ) *(u32x2*)(ga.o0 + (size_t)zt * DM + col) = u32x2{0u, 0u};
      } else if constexpr (MODE == 6) {
        const int col = cb * 192 + nt * 64 + wn * 16 + 4 * g;
        unsigned a0 = cvtpk(a[0], a[1]), a1 = cvtpk(a[2], a[3]);
        u32x2 v = {cvtpk(gelu_tanh(lo2f(a0)), gelu_tanh(hi2f(a0))), cvtpk(gelu_tanh(lo2f(a1)), gelu_tanh(hi2f(a1)))};
        if (col < FF) *(u32x2*)(ga.o0 + (size_t)tok * FF + col) = v;
      } else {
        const int col = cb * 64 + wn * 16 + 4 * g;
        u32x2 x1 = *(const u32x2*)(ga.r0 + (size_t)tok * DM + col);
        u32x2 hh = *(const u32x2*)(ga.r1 + (size_t)tok * DM + col);
        unsigned a0 = cvtpk(a[0], a[1]), a1 = cvtpk(a[2], a[3]);
        unsigned b0 = cvtpk(lo2f(x1[0]) + lo2f(a0), hi2f(x1[0]) + hi2f(a0));
        unsigned b1 = cvtpk(lo2f(x1[1]) + lo2f(a1), hi2f(x1[1]) + hi2f(a1));
        u32x2 v = {cvtpk(lo2f(hh[0]) + lo2f(b0), hi2f(hh[0]) + hi2f(b0)), cvtpk(lo2f(hh[1]) + lo2f(b1), hi2f(hh[1]) + hi2f(b1))};
        *(u32x2*)(ga.o0 + (size_t)tok * DM + col) = v;
      }
    }
  }
}

constexpr int AT_K = 0, AT_V = 65536, AT_ONES = 131072, AT_Q = AT_ONES + 8192, AT_LDS = AT_Q + 8192;

__global__ __launch_bounds__(512, 2) void attn_kernel(const u16* Qf, const u16* Kf, const u16* Vf, u16* O) {
  extern __shared__ __attribute__((aligned(16))) char lds[];
  const int tid = threadIdx.x, lane = tid & 63;
  const int wid = __builtin_amdgcn_readfirstlane(tid >> 6);
  const int xcd = blockIdx.x & 7, idx = blockIdx.x >> 3;
  const int head = xcd * 4 + (idx >> 3), qb = idx & 7;
  const unsigned lds0 = (unsigned)(uintptr_t)lds;
  const int r = lane & 31, h = lane >> 5;
  const char* kg = (const char*)Kf + (size_t)head * 65536;
  const char* vg = (const char*)Vf + (size_t)head * 65536;
  const char* qg = (const char*)Qf + (size_t)head * 65536 + (qb * 8 + wid) * 1024;
  const unsigned lo = lane * 16;

  glds16(qg, lo, lds0 + AT_Q + wid * 1024);
#pragma unroll
  for (int c = 0; c < 8; ++c) {
    glds16(kg, lo + c * 8192 + wid * 1024, lds0 + AT_K + c * 8192 + wid * 1024);
    glds16(vg, lo + c * 8192 + wid * 1024, lds0 + AT_V + c * 8192 + wid * 1024);
  }
  *(u32x4*)(lds + AT_ONES + tid * 16) = u32x4{0x3F803F80u, 0x3F803F80u, 0x3F803F80u, 0x3F803F80u};

  lds_cptr kp = (lds_cptr)lds + AT_K + lane * 16;
  lds_cptr vp = (lds_cptr)lds + ((r < 16) ? (AT_V + h * 256 + r * 16) : (AT_ONES + (r - 16) * 16));
  const int vstep = (r < 16) ? 4096 : 0;

  f32x16 o, negm;
#pragma unroll
  for (int i = 0; i < 16; ++i) { o[i] = 0.f; negm[i] = 0.f; }
  bf16x8 qf;

  auto tile = [&](lds_cptr kq, lds_cptr vq, bool first) {
    bf16x8 kf = lds_rd128(kq);
    bf16x8 vf0 = lds_rd128(vq), vf1 = lds_rd128(vq + 512);
    f32x16 s = MFMA32(kf, qf, negm);
    u32x4 pa, pb;
#pragma unroll
    for (int i = 0; i < 4; ++i) {
      pa[i] = cvtpk(__builtin_amdgcn_exp2f(s[2 * i]), __builtin_amdgcn_exp2f(s[2 * i + 1]));
      pb[i] = cvtpk(__builtin_amdgcn_exp2f(s[8 + 2 * i]), __builtin_amdgcn_exp2f(s[8 + 2 * i + 1]));
    }
    unsigned chk = (pa[0] | pa[1] | pa[2]) | (pa[3] | pb[0] | pb[1]) | (pb[2] | pb[3]);
    if (first || __any((chk & 0x40004000u) != 0)) {
      float mx = s[0];
#pragma unroll
      for (int i = 1; i < 16; ++i) mx = fmaxf(mx, s[i]);
      mx = fmaxf(mx, __shfl_xor(mx, 32));
      float d = mx + 7.0f;
      if (!first) {
        d = fmaxf(d, 0.f);
        float alpha = __builtin_amdgcn_exp2f(-d);
#pragma unroll
        for (int i = 0; i < 16; ++i) o[i] *= alpha;
      }
#pragma unroll
      for (int i = 0; i < 16; ++i) negm[i] -= d;
#pragma unroll
      for (int i = 0; i < 4; ++i) {
        pa[i] = cvtpk(__builtin_amdgcn_exp2f(s[2 * i] - d), __builtin_amdgcn_exp2f(s[2 * i + 1] - d));
        pb[i] = cvtpk(__builtin_amdgcn_exp2f(s[8 + 2 * i] - d), __builtin_amdgcn_exp2f(s[8 + 2 * i + 1] - d));
      }
    }
    o = MFMA32(vf0, __builtin_bit_cast(bf16x8, pa), o);
    o = MFMA32(vf1, __builtin_bit_cast(bf16x8, pb), o);
  };

  static_for<0, 8>([&](auto c_) {
    constexpr int c = c_.value;
    wait_vm_bar<2 * (7 - c)>();
    if constexpr (c == 0) qf = lds_rd128((lds_cptr)lds + AT_Q + wid * 1024 + lane * 16);
#pragma unroll
    for (int hc = 0; hc < 2; ++hc) {
#pragma unroll
      for (int t = 0; t < 4; ++t) tile(kp + t * 1024, vp + t * 1024, c == 0 && hc == 0 && t == 0);
      kp += 4096;
      vp += vstep;
    }
  });

  const float inv = 1.0f / o[8];
  const int q = qb * 256 + wid * 32 + r;
  u16* dst = O + (size_t)q * DM + head * 16 + 4 * h;
  *(u32x2*)dst = u32x2{cvtpk(o[0] * inv, o[1] * inv), cvtpk(o[2] * inv, o[3] * inv)};
  *(u32x2*)(dst + 8) = u32x2{cvtpk(o[4] * inv, o[5] * inv), cvtpk(o[6] * inv, o[7] * inv)};
}

template <int MODE>
static void launch_gemm(const GArgs& ga, hipStream_t stream) {
  hipFuncSetAttribute((const void*)gemm_kernel<MODE>, hipFuncAttributeMaxDynamicSharedMemorySize, Cfg<MODE>::LDS);
  gemm_kernel<MODE><<<256, 512, Cfg<MODE>::LDS, stream>>>(ga);
}

extern "C" void kernel_launch(void* const* d_in, const int* in_sizes, int n_in, void* d_out, int out_size, void* d_ws,
                              size_t ws_size, hipStream_t stream) {
  const u16* x = (const u16*)d_in[0];
  const u16* wqkv = (const u16*)d_in[1];
  const u16* wo = (const u16*)d_in[2];
  const u16* w1 = (const u16*)d_in[4];
  const u16* w2 = (const u16*)d_in[5];
  const u16* w3 = (const u16*)d_in[6];
  const u16* w4 = (const u16*)d_in[7];
  char* ws = (char*)d_ws;
  u16 *Q = (u16*)(ws + WS_Q), *Kf = (u16*)(ws + WS_K), *Vf = (u16*)(ws + WS_V), *O = (u16*)(ws + WS_O),
      *H = (u16*)(ws + WS_H), *Y = (u16*)(ws + WS_Y), *T = (u16*)(ws + WS_T), *X1 = (u16*)(ws + WS_X1),
      *U = (u16*)(ws + WS_U);
  u16* out = (u16*)d_out;

  launch_gemm<1>(GArgs{x, wqkv, nullptr, nullptr, Q, Kf, Vf}, stream);
  hipFuncSetAttribute((const void*)attn_kernel, hipFuncAttributeMaxDynamicSharedMemorySize, AT_LDS);
  attn_kernel<<<256, 512, AT_LDS, stream>>>(Q, Kf, Vf, O);
  launch_gemm<3>(GArgs{O, wo, x, nullptr, H, nullptr, nullptr}, stream);
  launch_gemm<4>(GArgs{H, w1, nullptr, nullptr, T, Y, nullptr}, stream);
  launch_gemm<5>(GArgs{T, w2, Y, nullptr, X1, nullptr, nullptr}, stream);
  launch_gemm<6>(GArgs{X1, w3, nullptr, nullptr, U, nullptr, nullptr}, stream);
  launch_gemm<7>(GArgs{U, w4, X1, H, out, nullptr, nullptr}, stream);
}
```

```cpp
#include <hip/hip_runtime.h>
#include <hip/hip_bf16.h>
#include <stdint.h>

typedef unsigned short u16;
typedef __attribute__((ext_vector_type(8))) short bf16x8;
typedef __attribute__((ext_vector_type(4))) short s16x4;
typedef __attribute__((ext_vector_type(2))) float f32x2;
typedef __attribute__((ext_vector_type(4))) float f32x4;
typedef __attribute__((ext_vector_type(16))) float f32x16;
typedef __attribute__((ext_vector_type(2))) __bf16 bf16x2_t;
typedef __attribute__((ext_vector_type(4))) unsigned u32x4;
typedef __attribute__((ext_vector_type(2))) unsigned u32x2;
typedef short v4i16_t __attribute__((ext_vector_type(4)));
typedef __attribute__((address_space(3))) const char* lds_cptr;

#define DEV __device__ __forceinline__
#define MFMA16(a, b, c) __builtin_amdgcn_mfma_f32_16x16x32_bf16((a), (b), (c), 0, 0, 0)
#define MFMA32(a, b, c) __builtin_amdgcn_mfma_f32_32x32x16_bf16((a), (b), (c), 0, 0, 0)

constexpr int SEQ = 2048, DM = 512, NH = 32, FF = 1376;

constexpr size_t WS_Q = 0, WS_K = 2u << 20, WS_V = 4u << 20, WS_O = 6u << 20, WS_H = 8u << 20, WS_PH = 10u << 20,
                 WS_PX = 11u << 20, WS_T = 12u << 20, WS_X1 = 14u << 20, WS_U = 16u << 20;

DEV unsigned cvtpk(float lo, float hi) {
  f32x2 v = {lo, hi};
  bf16x2_t b = __builtin_convertvector(v, bf16x2_t);
  return __builtin_bit_cast(unsigned, b);
}
DEV float lo2f(unsigned p) { return __uint_as_float(p << 16); }
DEV float hi2f(unsigned p) { return __uint_as_float(p & 0xffff0000u); }
DEV float rbf(float x) { return lo2f(cvtpk(x, x)); }

DEV void glds16(const void* gbase, unsigned voff, unsigned lds_base) {
  unsigned sv;
  asm volatile("s_mov_b32 %0, m0\n\ts_mov_b32 m0, %3\n\ts_nop 0\n\tglobal_load_lds_dwordx4 %1, %2\n\ts_mov_b32 m0, %0"
               : "=&s"(sv)
               : "v"(voff), "s"(gbase), "s"(lds_base)
               : "memory");
}
DEV u32x4 aload4(const void* gbase, unsigned voff) {
  u32x4 r;
  asm volatile("global_load_dwordx4 %0, %1, %2" : "=v"(r) : "v"(voff), "s"(gbase) : "memory");
  return r;
}
DEV u32x2 aload2(const void* gbase, unsigned voff) {
  u32x2 r;
  asm volatile("global_load_dwordx2 %0, %1, %2" : "=v"(r) : "v"(voff), "s"(gbase) : "memory");
  return r;
}
template <class T>
DEV void pin(T& x) { asm volatile("" : "+v"(x)); }
template <int N>
DEV void wait_vm_bar() {
  asm volatile("s_waitcnt vmcnt(%0) lgkmcnt(0)\n\ts_barrier" ::"i"(N) : "memory");
}
DEV void lds_bar() { asm volatile("s_waitcnt lgkmcnt(0)\n\ts_barrier" ::: "memory"); }
DEV bf16x8 lds_rd128(lds_cptr p) { return *(const __attribute__((address_space(3))) bf16x8*)p; }
DEV s16x4 lds_tr(lds_cptr p) {
  return __builtin_bit_cast(s16x4, __builtin_amdgcn_ds_read_tr16_b64_v4i16((__attribute__((address_space(3))) v4i16_t*)p));
}
template <int CTRL>
DEV float dpp_add(float v) {
  int x = __builtin_amdgcn_update_dpp(0, __float_as_int(v), CTRL, 0xf, 0xf, true);
  return v + __int_as_float(x);
}
DEV float add_x16(float v) {
  auto p = __builtin_amdgcn_permlane16_swap(__float_as_uint(v), __float_as_uint(v), false, false);
  return __uint_as_float(p[0]) + __uint_as_float(p[1]);
}
DEV float add_x32(float v) {
  auto p = __builtin_amdgcn_permlane32_swap(__float_as_uint(v), __float_as_uint(v), false, false);
  return __uint_as_float(p[0]) + __uint_as_float(p[1]);
}
DEV float max_x32(float v) {
  auto p = __builtin_amdgcn_permlane32_swap(__float_as_uint(v), __float_as_uint(v), false, false);
  return fmaxf(__uint_as_float(p[0]), __uint_as_float(p[1]));
}
DEV float wave_sum(float v) {
  v = dpp_add<0x121>(v);
  v = dpp_add<0x122>(v);
  v = dpp_add<0x124>(v);
  v = dpp_add<0x128>(v);
  return add_x32(add_x16(v));
}
DEV float dot2_ones(unsigned p, float acc) {
  return __builtin_amdgcn_fdot2_f32_bf16(__builtin_bit_cast(bf16x2_t, p), __builtin_bit_cast(bf16x2_t, 0x3F803F80u), acc, false);
}
DEV float gelu_tanh(float x) {
  const float c = -2.0f * 0.7978845608028654f * 1.4426950408889634f;
  float u = x * (1.0f + 0.044715f * x * x);
  float e = __builtin_amdgcn_exp2f(c * u);
  return x * __builtin_amdgcn_rcpf(1.0f + e);
}
DEV int bswz(int k) { return ((k >> 1) & 1) | (((k >> 3) & 1) << 1); }

#define STAMP(kid, i) do { } while (0)

template <int I, int N, class F>
DEV void static_for(F&& f) {
  if constexpr (I < N) {
    f(std::integral_constant<int, I>{});
    static_for<I + 1, N>(f);
  }
}

DEV void ln_consts(float s, float q, float& mb, float& sd, float& m, float& v) {
  m = s * (1.0f / DM);
  v = fmaxf(q * (1.0f / DM) - m * m, 0.f);
  mb = rbf(m);
  sd = rbf(__builtin_sqrtf(rbf(rbf(v) + 1e-5f)));
}

template <int MODE>
struct Cfg {
  static constexpr int NT = (MODE == 1 || MODE == 6) ? 3 : 1;
  static constexpr bool LN = (MODE == 1 || MODE == 4 || MODE == 6);
  static constexpr bool PST = (MODE == 4 || MODE == 5 || MODE == 6);
  static constexpr int K = (MODE == 7) ? FF : DM;
  static constexpr int LDA = K;
  static constexpr int LDW = (MODE == 1) ? 3 * DM : (MODE == 6 ? FF : DM);
  static constexpr int NSTAGE = (K + 63) / 64;
  static constexpr int RS = 128 * NT;
  static constexpr int BBYTES = 64 * RS;
  static constexpr int SLOT = BBYTES + 8192;
  static constexpr int NSLOT = (NT == 3) ? 4 : 9;
  static constexpr int NUSED = (NSLOT > NSTAGE ? NSTAGE : NSLOT);
  static constexpr int X_ST = NUSED * SLOT;
  static constexpr int X_CS = X_ST + 512;
  static constexpr int LDS = X_CS + 1536;
  static constexpr int LPS = NT + 1;
};

struct GArgs {
  const u16* A;
  const u16* W;
  const u16* r0;
  const u16* r1;
  const float* pin_;
  float* pout;
  u16* o0;
  u16* o1;
  u16* o2;
};

template <int MODE>
__global__ __launch_bounds__(512, 2) void gemm_kernel(GArgs ga) {
  using C = Cfg<MODE>;
  constexpr int NT = C::NT;
  extern __shared__ __attribute__((aligned(16))) char lds[];
  STAMP(MODE, 0);
  const int tid = threadIdx.x, lane = tid & 63;
  const int wid = __builtin_amdgcn_readfirstlane(tid >> 6);
  const int wm = wid & 1, wn = wid >> 1;
  const int xcd = blockIdx.x & 7, idx = blockIdx.x >> 3;
  const int rb = xcd * 4 + (idx >> 3), cb = idx & 7;
  const int row0 = rb * 64;
  const unsigned lds0 = (unsigned)(uintptr_t)lds;
  const int l15 = lane & 15, g = lane >> 4;

  u32x4 xr[MODE == 1 ? 8 : 1];
  u32x4 ps[2];
  u32x2 res0[2], res1[2];
  if constexpr (MODE == 1) {
#pragma unroll
    for (int i = 0; i < 8; ++i) xr[i] = aload4(ga.A, (unsigned)((row0 + wid * 8 + i) * DM + lane * 8) * 2u);
  }
  if constexpr (C::PST) {
    unsigned o = (unsigned)((row0 + (tid >> 3)) * 64 + (tid & 7) * 8) * 4u;
    ps[0] = aload4(ga.pin_, o);
    ps[1] = aload4(ga.pin_, o + 16);
  }
  if constexpr (MODE == 3 || MODE == 5 || MODE == 7) {
#pragma unroll
    for (int mt = 0; mt < 2; ++mt) {
      unsigned o = (unsigned)((row0 + wm * 32 + mt * 16 + l15) * DM + cb * 64 + wn * 16 + 4 * g) * 2u;
      res0[mt] = aload2(ga.r0, o);
      if constexpr (MODE == 7) res1[mt] = aload2(ga.r1, o);
    }
  }

  unsigned boff[NT];
  int bk[NT];
#pragma unroll
  for (int j = 0; j < NT; ++j) {
    int o = (wid * NT + j) * 1024 + lane * 16;
    int k = o / C::RS, rem = o % C::RS;
    int grp = rem >> 7, blk = (rem >> 5) & 3, hf = (rem >> 4) & 1;
    int col;
    if (MODE == 1) col = grp * 512 + cb * 64;
    else col = cb * (64 * NT) + grp * 64;
    col += ((blk ^ bswz(k)) * 16) + hf * 8;
    if (MODE == 6 && col > FF - 8) col = FF - 8;
    bk[j] = k;
    boff[j] = (unsigned)(k * C::LDW + col) * 2u;
  }
  unsigned aoff;
  {
    int r = wid * 8 + (lane >> 3), c = (lane & 7) ^ ((r >> 1) & 7);
    aoff = (unsigned)((row0 + r) * C::LDA + c * 8) * 2u;
  }
  auto issue = [&](int s) {
    const unsigned slot = lds0 + (unsigned)((s % C::NSLOT) * C::SLOT);
    glds16(ga.A, aoff + s * 128, slot + C::BBYTES + wid * 1024);
#pragma unroll
    for (int j = 0; j < NT; ++j) {
      unsigned off = boff[j] + (unsigned)(s * 64 * C::LDW) * 2u;
      if (MODE == 7 && s == C::NSTAGE - 1 && s * 64 + bk[j] >= FF) off = boff[j] + (unsigned)((FF - 64) * C::LDW) * 2u;
      glds16(ga.W, off, slot + (unsigned)(wid * NT + j) * 1024u);
    }
  };
  static_for<0, (C::NSLOT - 1 < C::NSTAGE ? C::NSLOT - 1 : C::NSTAGE)>([&](auto s_) { issue(s_.value); });

  int xoff[2][2];
#pragma unroll
  for (int mt = 0; mt < 2; ++mt)
#pragma unroll
    for (int kk = 0; kk < 2; ++kk) {
      int r = wm * 32 + mt * 16 + l15;
      xoff[mt][kk] = C::BBYTES + r * 128 + (((4 * kk + g) ^ ((r >> 1) & 7)) * 16);
    }
  int woff[2][2];
  {
    int q = l15 >> 2, p = l15 & 3;
#pragma unroll
    for (int kk = 0; kk < 2; ++kk)
#pragma unroll
      for (int t = 0; t < 2; ++t) {
        int k = 32 * kk + 8 * g + 4 * t + q;
        woff[kk][t] = k * C::RS + ((wn ^ bswz(k)) * 32) + p * 8;
      }
  }

  f32x4 acc[2][NT];
  float cs[NT];
#pragma unroll
  for (int nt = 0; nt < NT; ++nt) {
    cs[nt] = 0.f;
#pragma unroll
    for (int mt = 0; mt < 2; ++mt) acc[mt][nt] = f32x4{0.f, 0.f, 0.f, 0.f};
  }
  f32x2* st_lds = (f32x2*)(lds + C::X_ST);
  float* cs_lds = (float*)(lds + C::X_CS);

  static_for<0, C::NSTAGE>([&](auto s_) {
    constexpr int s = s_.value;
    constexpr int last_issued = (s + C::NSLOT - 2 < C::NSTAGE - 1) ? (s + C::NSLOT - 2) : (C::NSTAGE - 1);
    wait_vm_bar<(last_issued - s) * C::LPS>();
    if constexpr (s == 0) STAMP(MODE, 1);
    if constexpr (s + C::NSLOT - 1 < C::NSTAGE) issue(s + C::NSLOT - 1);
    const lds_cptr bs = (lds_cptr)lds + (s % C::NSLOT) * C::SLOT;
    constexpr int NKK = (MODE == 7 && s == C::NSTAGE - 1) ? 1 : 2;
#pragma unroll
    for (int kk = 0; kk < NKK; ++kk) {
      bf16x8 xf[2];
#pragma unroll
      for (int mt = 0; mt < 2; ++mt) xf[mt] = lds_rd128(bs + xoff[mt][kk]);
#pragma unroll
      for (int nt = 0; nt < NT; ++nt) {
        s16x4 w0 = lds_tr(bs + woff[kk][0] + nt * 128);
        s16x4 w1 = lds_tr(bs + woff[kk][1] + nt * 128);
        bf16x8 wf = __builtin_shufflevector(w0, w1, 0, 1, 2, 3, 4, 5, 6, 7);
#pragma unroll
        for (int mt = 0; mt < 2; ++mt) {
          if (MODE == 1 && nt == 2) acc[mt][nt] = MFMA16(xf[mt], wf, acc[mt][nt]);
          else acc[mt][nt] = MFMA16(wf, xf[mt], acc[mt][nt]);
        }
        if constexpr (C::LN) {
          if (wm == kk) {
            u32x4 wu = __builtin_bit_cast(u32x4, wf);
            cs[nt] = dot2_ones(wu[0], cs[nt]); cs[nt] = dot2_ones(wu[1], cs[nt]);
            cs[nt] = dot2_ones(wu[2], cs[nt]); cs[nt] = dot2_ones(wu[3], cs[nt]);
          }
        }
      }
    }
    if constexpr (s == 0) {
      if constexpr (MODE == 1) {
        float sm[8], sq[8];
#pragma unroll
        for (int i = 0; i < 8; ++i) {
          pin(xr[i]);
          float a = 0.f, b = 0.f;
#pragma unroll
          for (int e = 0; e < 4; ++e) {
            float x0 = lo2f(xr[i][e]), x1 = hi2f(xr[i][e]);
            a += x0 + x1; b = fmaf(x0, x0, fmaf(x1, x1, b));
          }
          sm[i] = a; sq[i] = b;
        }
#pragma unroll
        for (int i = 0; i < 8; ++i) { sm[i] = wave_sum(sm[i]); sq[i] = wave_sum(sq[i]); }
        float s_ = sm[0], q_ = sq[0];
#pragma unroll
        for (int i = 1; i < 8; ++i) if (lane == i) { s_ = sm[i]; q_ = sq[i]; }
        float mb, sd, m, v;
        ln_consts(s_, q_, mb, sd, m, v);
        if (lane < 8) st_lds[wid * 8 + lane] = f32x2{mb, 1.0f / sd};
      }
      if constexpr (C::PST) {
        pin(ps[0]); pin(ps[1]);
        float s_ = (__uint_as_float(ps[0][0]) + __uint_as_float(ps[0][2])) + (__uint_as_float(ps[1][0]) + __uint_as_float(ps[1][2]));
        float q_ = (__uint_as_float(ps[0][1]) + __uint_as_float(ps[0][3])) + (__uint_as_float(ps[1][1]) + __uint_as_float(ps[1][3]));
        s_ = dpp_add<0xB1>(s_); q_ = dpp_add<0xB1>(q_);
        s_ = dpp_add<0x4E>(s_); q_ = dpp_add<0x4E>(q_);
        s_ = dpp_add<0x141>(s_); q_ = dpp_add<0x141>(q_);
        float mb, sd, m, v;
        ln_consts(s_, q_, mb, sd, m, v);
        float M = mb, R = 1.0f / sd;
        if constexpr (MODE == 4) {
          float m2 = (m - mb) * R, v2 = v * R * R;
          float m2b = rbf(m2);
          float sd2 = rbf(__builtin_sqrtf(rbf(rbf(v2) + 1e-5f)));
          M = mb + m2b * sd;
          R = R / sd2;
        }
        if ((tid & 7) == 0) st_lds[tid >> 3] = f32x2{M, R};
      }
    }
  });

  if constexpr (C::LN) {
#pragma unroll
    for (int nt = 0; nt < NT; ++nt) {
      float c = add_x32(add_x16(cs[nt]));
      if (g == 0) cs_lds[wm * 192 + nt * 64 + wn * 16 + l15] = c;
    }
    lds_bar();
  }
  STAMP(MODE, 2);
#pragma unroll
  for (int mt = 0; mt < 2; ++mt) {
    const int lrow = wm * 32 + mt * 16 + l15;
    const int tok = row0 + lrow;
#pragma unroll
    for (int nt = 0; nt < NT; ++nt) {
      f32x4 a = acc[mt][nt];
      if constexpr (C::LN) {
        if (MODE == 1 && nt == 2) {
          float c = cs_lds[nt * 64 + wn * 16 + l15] + cs_lds[192 + nt * 64 + wn * 16 + l15];
#pragma unroll
          for (int i = 0; i < 4; ++i) {
            f32x2 st = st_lds[wm * 32 + mt * 16 + 4 * g + i];
            a[i] = (a[i] - st[0] * c) * st[1];
          }
        } else {
          f32x4 c = *(const f32x4*)(cs_lds + nt * 64 + wn * 16 + 4 * g) + *(const f32x4*)(cs_lds + 192 + nt * 64 + wn * 16 + 4 * g);
          f32x2 st = st_lds[lrow];
#pragma unroll
          for (int i = 0; i < 4; ++i) a[i] = (a[i] - st[0] * c[i]) * st[1];
        }
      }
      if constexpr (MODE == 1) {
        const int head = cb * 4 + wn;
        if (nt < 2) {
          float sc = (nt == 0) ? 0.25f * 1.4426950408889634f : 1.0f;
          u32x2 v = {cvtpk(a[0] * sc, a[1] * sc), cvtpk(a[2] * sc, a[3] * sc)};
          u16* dst = (nt == 0 ? ga.o0 : ga.o1) + ((size_t)(head * 64 + (tok >> 5)) * 2 + (g >> 1)) * 256 + (tok & 31) * 8 + (g & 1) * 4;
          *(u32x2*)dst = v;
        } else {
          const int step = (row0 + wm * 32 + mt * 16) >> 4;
          u32x2 v = {cvtpk(a[0], a[1]), cvtpk(a[2], a[3])};
          u16* dst = ga.o2 + ((size_t)(head * 128 + step) * 2 + (g & 1)) * 128 + l15 * 8 + (g >> 1) * 4;
          *(u32x2*)dst = v;
        }
      } else if constexpr (MODE == 3 || MODE == 5) {
        const int col = cb * 64 + wn * 16 + 4 * g;
        pin(res0[mt]);
        u32x2 r = res0[mt];
        if constexpr (MODE == 5) {
          f32x2 st = st_lds[lrow];
          unsigned d0 = cvtpk(lo2f(r[0]) - st[0], hi2f(r[0]) - st[0]), d1 = cvtpk(lo2f(r[1]) - st[0], hi2f(r[1]) - st[0]);
          r[0] = cvtpk(lo2f(d0) * st[1], hi2f(d0) * st[1]);
          r[1] = cvtpk(lo2f(d1) * st[1], hi2f(d1) * st[1]);
        }
        unsigned a0 = cvtpk(a[0], a[1]), a1 = cvtpk(a[2], a[3]);
        u32x2 v = {cvtpk(lo2f(r[0]) + lo2f(a0), hi2f(r[0]) + hi2f(a0)), cvtpk(lo2f(r[1]) + lo2f(a1), hi2f(r[1]) + hi2f(a1))};
        *(u32x2*)(ga.o0 + (size_t)tok * DM + col) = v;
        float f0 = lo2f(v[0]), f1 = hi2f(v[0]), f2 = lo2f(v[1]), f3 = hi2f(v[1]);
        float s_ = (f0 + f1) + (f2 + f3), q_ = fmaf(f0, f0, f1 * f1) + fmaf(f2, f2, f3 * f3);
        s_ = add_x32(add_x16(s_)); q_ = add_x32(add_x16(q_));
        if (g == 0) *(f32x2*)(ga.pout + (size_t)tok * 64 + (cb * 4 + wn) * 2) = f32x2{s_, q_};
      } else if constexpr (MODE == 4) {
        const int col = cb * 64 + wn * 16 + 4 * g;
        unsigned a0 = cvtpk(a[0], a[1]), a1 = cvtpk(a[2], a[3]);
        u32x2 v = {cvtpk(gelu_tanh(lo2f(a0)), gelu_tanh(hi2f(a0))), cvtpk(gelu_tanh(lo2f(a1)), gelu_tanh(hi2f(a1)))};
        const int grp = cb >> 1;
        const int sh = (grp == 0) ? 1 : (grp == 1) ? -1 : (grp == 2) ? 2 : -2;
        const int dt = tok + sh;
        if (dt >= 0 && dt < SEQ) *(u32x2*)(ga.o0 + (size_t)dt * DM + col) = v;
        const int src = tok - sh;
        if (src < 0 || src >= SEQ) *(u32x2*)(ga.o0 + (size_t)tok * DM + col) = u32x2{0u, 0u};
      } else if constexpr (MODE == 6) {
        const int col = cb * 192 + nt * 64 + wn * 16 + 4 * g;
        unsigned a0 = cvtpk(a[0], a[1]), a1 = cvtpk(a[2], a[3]);
        u32x2 v = {cvtpk(gelu_tanh(lo2f(a0)), gelu_tanh(hi2f(a0))), cvtpk(gelu_tanh(lo2f(a1)), gelu_tanh(hi2f(a1)))};
        if (col < FF) *(u32x2*)(ga.o0 + (size_t)tok * FF + col) = v;
      } else {
        const int col = cb * 64 + wn * 16 + 4 * g;
        pin(res0[mt]); pin(res1[mt]);
        u32x2 x1 = res0[mt], hh = res1[mt];
        unsigned a0 = cvtpk(a[0], a[1]), a1 = cvtpk(a[2], a[3]);
        unsigned b0 = cvtpk(lo2f(x1[0]) + lo2f(a0), hi2f(x1[0]) + hi2f(a0));
        unsigned b1 = cvtpk(lo2f(x1[1]) + lo2f(a1), hi2f(x1[1]) + hi2f(a1));
        u32x2 v = {cvtpk(lo2f(hh[0]) + lo2f(b0), hi2f(hh[0]) + hi2f(b0)), cvtpk(lo2f(hh[1]) + lo2f(b1), hi2f(hh[1]) + hi2f(b1))};
        *(u32x2*)(ga.o0 + (size_t)tok * DM + col) = v;
      }
    }
  }
  STAMP(MODE, 3);
}

constexpr int AT_K = 0, AT_V = 65536, AT_ONES = 131072, AT_Q = AT_ONES + 8192, AT_LDS = AT_Q + 8192;

__global__ __launch_bounds__(512, 2) void attn_kernel(const u16* Qf, const u16* Kf, const u16* Vf, u16* O) {
  extern __shared__ __attribute__((aligned(16))) char lds[];
  STAMP(2, 0);
  const int tid = threadIdx.x, lane = tid & 63;
  const int wid = __builtin_amdgcn_readfirstlane(tid >> 6);
  const int xcd = blockIdx.x & 7, idx = blockIdx.x >> 3;
  const int head = xcd * 4 + (idx >> 3), qb = idx & 7;
  const unsigned lds0 = (unsigned)(uintptr_t)lds;
  const int r = lane & 31, h = lane >> 5;
  const char* kg = (const char*)Kf + (size_t)head * 65536;
  const char* vg = (const char*)Vf + (size_t)head * 65536;
  const char* qg = (const char*)Qf + (size_t)head * 65536 + (qb * 8 + wid) * 1024;
  const unsigned lo = lane * 16;

  glds16(qg, lo, lds0 + AT_Q + wid * 1024);
#pragma unroll
  for (int c = 0; c < 8; ++c) {
    glds16(kg, lo + c * 8192 + wid * 1024, lds0 + AT_K + c * 8192 + wid * 1024);
    glds16(vg, lo + c * 8192 + wid * 1024, lds0 + AT_V + c * 8192 + wid * 1024);
  }
  *(u32x4*)(lds + AT_ONES + tid * 16) = u32x4{0x3F803F80u, 0x3F803F80u, 0x3F803F80u, 0x3F803F80u};

  lds_cptr kp = (lds_cptr)lds + AT_K + lane * 16;
  lds_cptr vp = (lds_cptr)lds + ((r < 16) ? (AT_V + h * 256 + r * 16) : (AT_ONES + (r - 16) * 16));
  const int vstep = (r < 16) ? 4096 : 0;

  f32x16 o, negm, sA, sB;
#pragma unroll
  for (int i = 0; i < 16; ++i) { o[i] = 0.f; negm[i] = 0.f; }

  wait_vm_bar<10>();
  STAMP(2, 1);
  const bf16x8 qf = lds_rd128((lds_cptr)lds + AT_Q + wid * 1024 + lane * 16);
  {
    sA = MFMA32(lds_rd128(kp), qf, negm);
    float mx = sA[0];
#pragma unroll
    for (int i = 1; i < 16; ++i) mx = fmaxf(mx, sA[i]);
    const float d = max_x32(mx) + 7.0f;
#pragma unroll
    for (int i = 0; i < 16; ++i) { negm[i] = -d; sA[i] -= d; }
  }

  auto step = [&](f32x16& sc, f32x16& sn, lds_cptr kn, lds_cptr vq) {
    bf16x8 kf = lds_rd128(kn);
    bf16x8 vf0 = lds_rd128(vq), vf1 = lds_rd128(vq + 512);
    sn = MFMA32(kf, qf, negm);
    u32x4 pa, pb;
#pragma unroll
    for (int i = 0; i < 4; ++i) {
      pa[i] = cvtpk(__builtin_amdgcn_exp2f(sc[2 * i]), __builtin_amdgcn_exp2f(sc[2 * i + 1]));
      pb[i] = cvtpk(__builtin_amdgcn_exp2f(sc[8 + 2 * i]), __builtin_amdgcn_exp2f(sc[8 + 2 * i + 1]));
    }
    unsigned chk = (pa[0] | pa[1] | pa[2]) | (pa[3] | pb[0] | pb[1]) | (pb[2] | pb[3]);
    if (__builtin_expect(__any((chk & 0x40004000u) != 0), 0)) {
      float mx = sc[0];
#pragma unroll
      for (int i = 1; i < 16; ++i) mx = fmaxf(mx, sc[i]);
      const float d = fmaxf(max_x32(mx) + 7.0f, 0.f);
      const float alpha = __builtin_amdgcn_exp2f(-d);
#pragma unroll
      for (int i = 0; i < 16; ++i) { o[i] *= alpha; negm[i] -= d; sn[i] -= d; }
#pragma unroll
      for (int i = 0; i < 4; ++i) {
        pa[i] = cvtpk(__builtin_amdgcn_exp2f(sc[2 * i] - d), __builtin_amdgcn_exp2f(sc[2 * i + 1] - d));
        pb[i] = cvtpk(__builtin_amdgcn_exp2f(sc[8 + 2 * i] - d), __builtin_amdgcn_exp2f(sc[8 + 2 * i + 1] - d));
      }
    }
    o = MFMA32(vf0, __builtin_bit_cast(bf16x8, pa), o);
    o = MFMA32(vf1, __builtin_bit_cast(bf16x8, pb), o);
  };
  auto group = [&]() {
    step(sA, sB, kp + 1024, vp);
    step(sB, sA, kp + 2048, vp + 1024);
    step(sA, sB, kp + 3072, vp + 2048);
    step(sB, sA, kp + 4096, vp + 3072);
    kp += 4096;
    vp += vstep;
  };
#pragma unroll 1
  for (int it = 0; it < 4; ++it) group();
  wait_vm_bar<0>();
#pragma unroll 1
  for (int it = 4; it < 16; ++it) group();

  STAMP(2, 2);
  const float inv = 1.0f / o[8];
  const int q = qb * 256 + wid * 32 + r;
  u16* dst = O + (size_t)q * DM + head * 16 + 4 * h;
  *(u32x2*)dst = u32x2{cvtpk(o[0] * inv, o[1] * inv), cvtpk(o[2] * inv, o[3] * inv)};
  *(u32x2*)(dst + 8) = u32x2{cvtpk(o[4] * inv, o[5] * inv), cvtpk(o[6] * inv, o[7] * inv)};
  STAMP(2, 3);
}

template <int MODE>
static void launch_gemm(const GArgs& ga, hipStream_t stream) {
  (void)hipFuncSetAttribute((const void*)gemm_kernel<MODE>, hipFuncAttributeMaxDynamicSharedMemorySize, Cfg<MODE>::LDS);
  gemm_kernel<MODE><<<256, 512, Cfg<MODE>::LDS, stream>>>(ga);
}

extern "C" void kernel_launch(void* const* d_in, const int* in_sizes, int n_in, void* d_out, int out_size, void* d_ws,
                              size_t ws_size, hipStream_t stream) {
  const u16* x = (const u16*)d_in[0];
  const u16* wqkv = (const u16*)d_in[1];
  const u16* wo = (const u16*)d_in[2];
  const u16* w1 = (const u16*)d_in[4];
  const u16* w2 = (const u16*)d_in[5];
  const u16* w3 = (const u16*)d_in[6];
  const u16* w4 = (const u16*)d_in[7];
  char* ws = (char*)d_ws;
  u16 *Q = (u16*)(ws + WS_Q), *Kf = (u16*)(ws + WS_K), *Vf = (u16*)(ws + WS_V), *O = (u16*)(ws + WS_O),
      *H = (u16*)(ws + WS_H), *T = (u16*)(ws + WS_T), *X1 = (u16*)(ws + WS_X1), *U = (u16*)(ws + WS_U);
  float *PH = (float*)(ws + WS_PH), *PX = (float*)(ws + WS_PX);
  u16* out = (u16*)d_out;

  launch_gemm<1>(GArgs{x, wqkv, nullptr, nullptr, nullptr, nullptr, Q, Kf, Vf}, stream);
  (void)hipFuncSetAttribute((const void*)attn_kernel, hipFuncAttributeMaxDynamicSharedMemorySize, AT_LDS);
  attn_kernel<<<256, 512, AT_LDS, stream>>>(Q, Kf, Vf, O);
  launch_gemm<3>(GArgs{O, wo, x, nullptr, nullptr, PH, H, nullptr, nullptr}, stream);
  launch_gemm<4>(GArgs{H, w1, nullptr, nullptr, PH, nullptr, T, nullptr, nullptr}, stream);
  launch_gemm<5>(GArgs{T, w2, H, nullptr, PH, PX, X1, nullptr, nullptr}, stream);
  launch_gemm<6>(GArgs{X1, w3, nullptr, nullptr, PX, nullptr, U, nullptr, nullptr}, stream);
  launch_gemm<7>(GArgs{U, w4, X1, H, nullptr, nullptr, out, nullptr, nullptr}, stream);
}
```

```cpp
#include <hip/hip_runtime.h>
#include <hip/hip_bf16.h>
#include <stdint.h>

typedef unsigned short u16;
typedef __attribute__((ext_vector_type(8))) short bf16x8;
typedef __attribute__((ext_vector_type(4))) short s16x4;
typedef __attribute__((ext_vector_type(2))) float f32x2;
typedef __attribute__((ext_vector_type(4))) float f32x4;
typedef __attribute__((ext_vector_type(16))) float f32x16;
typedef __attribute__((ext_vector_type(2))) __bf16 bf16x2_t;
typedef __attribute__((ext_vector_type(4))) unsigned u32x4;
typedef __attribute__((ext_vector_type(2))) unsigned u32x2;
typedef short v4i16_t __attribute__((ext_vector_type(4)));
typedef __attribute__((address_space(3))) const char* lds_cptr;

#define DEV __device__ __forceinline__
#define MFMA16(a, b, c) __builtin_amdgcn_mfma_f32_16x16x32_bf16((a), (b), (c), 0, 0, 0)
#define MFMA32(a, b, c) __builtin_amdgcn_mfma_f32_32x32x16_bf16((a), (b), (c), 0, 0, 0)

constexpr int SEQ = 2048, DM = 512, NH = 32, FF = 1376;

constexpr size_t WS_Q = 0, WS_K = 2u << 20, WS_V = 4u << 20, WS_O = 6u << 20, WS_H = 8u << 20, WS_PH = 10u << 20,
                 WS_PX = 11u << 20, WS_T = 12u << 20, WS_X1 = 14u << 20, WS_U = 16u << 20;

DEV unsigned cvtpk(float lo, float hi) {
  f32x2 v = {lo, hi};
  bf16x2_t b = __builtin_convertvector(v, bf16x2_t);
  return __builtin_bit_cast(unsigned, b);
}
DEV float lo2f(unsigned p) { return __uint_as_float(p << 16); }
DEV float hi2f(unsigned p) { return __uint_as_float(p & 0xffff0000u); }
DEV float rbf(float x) { return lo2f(cvtpk(x, x)); }

DEV void glds16(const void* gbase, unsigned voff, unsigned lds_base) {
  unsigned sv;
  asm volatile("s_mov_b32 %0, m0\n\ts_mov_b32 m0, %3\n\ts_nop 0\n\tglobal_load_lds_dwordx4 %1, %2\n\ts_mov_b32 m0, %0"
               : "=&s"(sv)
               : "v"(voff), "s"(gbase), "s"(lds_base)
               : "memory");
}
DEV u32x4 aload4(const void* gbase, unsigned voff) {
  u32x4 r;
  asm volatile("global_load_dwordx4 %0, %1, %2" : "=v"(r) : "v"(voff), "s"(gbase) : "memory");
  return r;
}
DEV u32x2 aload2(const void* gbase, unsigned voff) {
  u32x2 r;
  asm volatile("global_load_dwordx2 %0, %1, %2" : "=v"(r) : "v"(voff), "s"(gbase) : "memory");
  return r;
}
template <class T>
DEV void pin(T& x) { asm volatile("" : "+v"(x)); }
template <int N>
DEV void wait_vm_bar() {
  asm volatile("s_waitcnt vmcnt(%0) lgkmcnt(0)\n\ts_barrier" ::"i"(N) : "memory");
}
DEV void lds_bar() { asm volatile("s_waitcnt lgkmcnt(0)\n\ts_barrier" ::: "memory"); }
DEV bf16x8 lds_rd128(lds_cptr p) { return *(const __attribute__((address_space(3))) bf16x8*)p; }
DEV s16x4 lds_tr(lds_cptr p) {
  return __builtin_bit_cast(s16x4, __builtin_amdgcn_ds_read_tr16_b64_v4i16((__attribute__((address_space(3))) v4i16_t*)p));
}
template <int CTRL>
DEV float dpp_add(float v) {
  int x = __builtin_amdgcn_update_dpp(0, __float_as_int(v), CTRL, 0xf, 0xf, true);
  return v + __int_as_float(x);
}
DEV float add_x16(float v) {
  auto p = __builtin_amdgcn_permlane16_swap(__float_as_uint(v), __float_as_uint(v), false, false);
  return __uint_as_float(p[0]) + __uint_as_float(p[1]);
}
DEV float add_x32(float v) {
  auto p = __builtin_amdgcn_permlane32_swap(__float_as_uint(v), __float_as_uint(v), false, false);
  return __uint_as_float(p[0]) + __uint_as_float(p[1]);
}
DEV float max_x32(float v) {
  auto p = __builtin_amdgcn_permlane32_swap(__float_as_uint(v), __float_as_uint(v), false, false);
  return fmaxf(__uint_as_float(p[0]), __uint_as_float(p[1]));
}
DEV float wave_sum(float v) {
  v = dpp_add<0x121>(v);
  v = dpp_add<0x122>(v);
  v = dpp_add<0x124>(v);
  v = dpp_add<0x128>(v);
  return add_x32(add_x16(v));
}
DEV float dot2_ones(unsigned p, float acc) {
  return __builtin_amdgcn_fdot2_f32_bf16(__builtin_bit_cast(bf16x2_t, p), __builtin_bit_cast(bf16x2_t, 0x3F803F80u), acc, false);
}
DEV float gelu_tanh(float x) {
  const float c = -2.0f * 0.7978845608028654f * 1.4426950408889634f;
  float u = x * (1.0f + 0.044715f * x * x);
  float e = __builtin_amdgcn_exp2f(c * u);
  return x * __builtin_amdgcn_rcpf(1.0f + e);
}
DEV int bswz(int k) { return ((k >> 1) & 1) | (((k >> 3) & 1) << 1); }

#define STAMP(kid, i) do { } while (0)

template <int I, int N, class F>
DEV void static_for(F&& f) {
  if constexpr (I < N) {
    f(std::integral_constant<int, I>{});
    static_for<I + 1, N>(f);
  }
}

DEV void ln_consts(float s, float q, float& mb, float& sd, float& m, float& v) {
  m = s * (1.0f / DM);
  v = fmaxf(q * (1.0f / DM) - m * m, 0.f);
  mb = rbf(m);
  sd = rbf(__builtin_sqrtf(rbf(rbf(v) + 1e-5f)));
}

template <int MODE>
struct Cfg {
  static constexpr int NT = (MODE == 1 || MODE == 6) ? 3 : 1;
  static constexpr bool LN = (MODE == 1 || MODE == 4 || MODE == 6);
  static constexpr bool PST = (MODE == 4 || MODE == 5 || MODE == 6);
  static constexpr int K = (MODE == 7) ? FF : DM;
  static constexpr int LDA = K;
  static constexpr int LDW = (MODE == 1) ? 3 * DM : (MODE == 6 ? FF : DM);
  static constexpr int NSTAGE = (K + 63) / 64;
  static constexpr int SPB = (NT == 1) ? 2 : 1;
  static constexpr int NB = (NSTAGE + SPB - 1) / SPB;
  static constexpr int RS = 128 * NT;
  static constexpr int BBYTES = 64 * RS;
  static constexpr int SLOT = BBYTES + 8192;
  static constexpr int NGSLOT = 4;
  static constexpr int NSLOT = NGSLOT * SPB;
  static constexpr int DEPTH = (NB <= NGSLOT) ? NB : NGSLOT - 1;
  static constexpr int X_ST = NSLOT * SLOT;
  static constexpr int X_CS = X_ST + 512;
  static constexpr int LDS = X_CS + 1536;
  static constexpr int LPS = 4 * (NT + 1);
};

struct GArgs {
  const u16* A;
  const u16* W;
  const u16* r0;
  const u16* r1;
  const float* pin_;
  float* pout;
  u16* o0;
  u16* o1;
  u16* o2;
};

template <int MODE>
__global__ __launch_bounds__(640, 2) void gemm_kernel(GArgs ga) {
  using C = Cfg<MODE>;
  constexpr int NT = C::NT;
  extern __shared__ __attribute__((aligned(16))) char lds[];
  STAMP(MODE, 0);
  const int tid = threadIdx.x, lane = tid & 63;
  const int wid = __builtin_amdgcn_readfirstlane(tid >> 6);
  const int xcd = blockIdx.x & 7, idx = blockIdx.x >> 3;
  const int rb = xcd * 4 + (idx >> 3), cb = idx & 7;
  const int row0 = rb * 64;
  const unsigned lds0 = (unsigned)(uintptr_t)lds;

  if (wid >= 8) {
    const int L = wid - 8;
    unsigned aoff;
    {
      int r = L * 32 + (lane >> 3), c = (lane & 7) ^ ((r >> 1) & 7);
      aoff = (unsigned)((row0 + r) * C::LDA + c * 8) * 2u;
    }
    unsigned boff[4 * NT];
    int bk[4 * NT];
#pragma unroll
    for (int j = 0; j < 4 * NT; ++j) {
      int o = (L * 4 * NT + j) * 1024 + lane * 16;
      int k = o / C::RS, rem = o % C::RS;
      int grp = rem >> 7, blk = (rem >> 5) & 3, hf = (rem >> 4) & 1;
      int col;
      if (MODE == 1) col = grp * 512 + cb * 64;
      else col = cb * (64 * NT) + grp * 64;
      col += ((blk ^ bswz(k)) * 16) + hf * 8;
      if (MODE == 6 && col > FF - 8) col = FF - 8;
      bk[j] = k;
      boff[j] = (unsigned)(k * C::LDW + col) * 2u;
    }
    unsigned aoffj[4];
#pragma unroll
    for (int j = 0; j < 4; ++j) {
      int r = L * 32 + j * 8 + (lane >> 3), c = (lane & 7) ^ ((r >> 1) & 7);
      aoffj[j] = (unsigned)((row0 + r) * C::LDA + c * 8) * 2u;
    }
    (void)aoff;
    auto issue_sub = [&](int s) {
      const unsigned slot = lds0 + (unsigned)((s % C::NSLOT) * C::SLOT);
#pragma unroll
      for (int j = 0; j < 4; ++j) glds16(ga.A, aoffj[j] + s * 128, slot + C::BBYTES + (L * 4 + j) * 1024);
#pragma unroll
      for (int j = 0; j < 4 * NT; ++j) {
        unsigned off = boff[j] + (unsigned)(s * 64 * C::LDW) * 2u;
        if (MODE == 7 && s == C::NSTAGE - 1 && s * 64 + bk[j] >= FF) off = boff[j] + (unsigned)((FF - 64) * C::LDW) * 2u;
        glds16(ga.W, off, slot + (unsigned)(L * 4 * NT + j) * 1024u);
      }
    };
    auto nsub = [](int b) { int e = (b + 1) * C::SPB; return (e > C::NSTAGE ? C::NSTAGE : e) - b * C::SPB; };
    static_for<0, C::DEPTH>([&](auto b_) {
      static_for<0, C::SPB>([&](auto u_) { if constexpr (b_.value * C::SPB + u_.value < C::NSTAGE) issue_sub(b_.value * C::SPB + u_.value); });
    });
    static_for<0, C::NB>([&](auto b_) {
      constexpr int b = b_.value;
      constexpr int lastg = (b + C::DEPTH - 1 < C::NB - 1) ? (b + C::DEPTH - 1) : (C::NB - 1);
      constexpr int sub_after = ((lastg + 1) * C::SPB > C::NSTAGE ? C::NSTAGE : (lastg + 1) * C::SPB) - ((b + 1) * C::SPB > C::NSTAGE ? C::NSTAGE : (b + 1) * C::SPB);
      wait_vm_bar<sub_after * C::LPS>();
      if constexpr (b + C::DEPTH < C::NB) {
        static_for<0, C::SPB>([&](auto u_) {
          if constexpr ((b + C::DEPTH) * C::SPB + u_.value < C::NSTAGE) issue_sub((b + C::DEPTH) * C::SPB + u_.value);
        });
      }
    });
    (void)nsub;
    return;
  }

  const int wm = wid & 1, wn = wid >> 1;
  const int l15 = lane & 15, g = lane >> 4;
  u32x4 xr[MODE == 1 ? 8 : 1];
  u32x4 ps[2];
  u32x2 res0[2], res1[2];
  if constexpr (MODE == 1) {
#pragma unroll
    for (int i = 0; i < 8; ++i) xr[i] = *(const u32x4*)(ga.A + (size_t)(row0 + wid * 8 + i) * DM + lane * 8);
  }
  if constexpr (C::PST) {
    const float* pp = ga.pin_ + (size_t)(row0 + (tid >> 3)) * 64 + (tid & 7) * 8;
    ps[0] = *(const u32x4*)pp;
    ps[1] = *(const u32x4*)(pp + 4);
  }
  if constexpr (MODE == 3 || MODE == 5 || MODE == 7) {
#pragma unroll
    for (int mt = 0; mt < 2; ++mt) {
      size_t o = (size_t)(row0 + wm * 32 + mt * 16 + l15) * DM + cb * 64 + wn * 16 + 4 * g;
      res0[mt] = *(const u32x2*)(ga.r0 + o);
      if constexpr (MODE == 7) res1[mt] = *(const u32x2*)(ga.r1 + o);
    }
  }

  int xoff[2][2];
#pragma unroll
  for (int mt = 0; mt < 2; ++mt)
#pragma unroll
    for (int kk = 0; kk < 2; ++kk) {
      int r = wm * 32 + mt * 16 + l15;
      xoff[mt][kk] = C::BBYTES + r * 128 + (((4 * kk + g) ^ ((r >> 1) & 7)) * 16);
    }
  int woff[2][2];
  {
    int q = l15 >> 2, p = l15 & 3;
#pragma unroll
    for (int kk = 0; kk < 2; ++kk)
#pragma unroll
      for (int t = 0; t < 2; ++t) {
        int k = 32 * kk + 8 * g + 4 * t + q;
        woff[kk][t] = k * C::RS + ((wn ^ bswz(k)) * 32) + p * 8;
      }
  }

  f32x4 acc[2][NT];
  float cs[NT];
#pragma unroll
  for (int nt = 0; nt < NT; ++nt) {
    cs[nt] = 0.f;
#pragma unroll
    for (int mt = 0; mt < 2; ++mt) acc[mt][nt] = f32x4{0.f, 0.f, 0.f, 0.f};
  }
  f32x2* st_lds = (f32x2*)(lds + C::X_ST);
  float* cs_lds = (float*)(lds + C::X_CS);

  static_for<0, C::NSTAGE>([&](auto s_) {
    constexpr int s = s_.value;
    if constexpr (s % C::SPB == 0) lds_bar();
    if constexpr (s == 0) STAMP(MODE, 1);
    const lds_cptr bs = (lds_cptr)lds + (s % C::NSLOT) * C::SLOT;
    constexpr int NKK = (MODE == 7 && s == C::NSTAGE - 1) ? 1 : 2;
#pragma unroll
    for (int kk = 0; kk < NKK; ++kk) {
      bf16x8 xf[2];
#pragma unroll
      for (int mt = 0; mt < 2; ++mt) xf[mt] = lds_rd128(bs + xoff[mt][kk]);
#pragma unroll
      for (int nt = 0; nt < NT; ++nt) {
        s16x4 w0 = lds_tr(bs + woff[kk][0] + nt * 128);
        s16x4 w1 = lds_tr(bs + woff[kk][1] + nt * 128);
        bf16x8 wf = __builtin_shufflevector(w0, w1, 0, 1, 2, 3, 4, 5, 6, 7);
#pragma unroll
        for (int mt = 0; mt < 2; ++mt) {
          if (MODE == 1 && nt == 2) acc[mt][nt] = MFMA16(xf[mt], wf, acc[mt][nt]);
          else acc[mt][nt] = MFMA16(wf, xf[mt], acc[mt][nt]);
        }
        if constexpr (C::LN) {
          if (wm == kk) {
            u32x4 wu = __builtin_bit_cast(u32x4, wf);
            cs[nt] = dot2_ones(wu[0], cs[nt]); cs[nt] = dot2_ones(wu[1], cs[nt]);
            cs[nt] = dot2_ones(wu[2], cs[nt]); cs[nt] = dot2_ones(wu[3], cs[nt]);
          }
        }
      }
    }
    if constexpr (s == 0) {
      if constexpr (MODE == 1) {
        float sm[8], sq[8];
#pragma unroll
        for (int i = 0; i < 8; ++i) {
          float a = 0.f, b = 0.f;
#pragma unroll
          for (int e = 0; e < 4; ++e) {
            float x0 = lo2f(xr[i][e]), x1 = hi2f(xr[i][e]);
            a += x0 + x1; b = fmaf(x0, x0, fmaf(x1, x1, b));
          }
          sm[i] = a; sq[i] = b;
        }
#pragma unroll
        for (int i = 0; i < 8; ++i) { sm[i] = wave_sum(sm[i]); sq[i] = wave_sum(sq[i]); }
        float s_ = sm[0], q_ = sq[0];
#pragma unroll
        for (int i = 1; i < 8; ++i) if (lane == i) { s_ = sm[i]; q_ = sq[i]; }
        float mb, sd, m, v;
        ln_consts(s_, q_, mb, sd, m, v);
        if (lane < 8) st_lds[wid * 8 + lane] = f32x2{mb, 1.0f / sd};
      }
      if constexpr (C::PST) {
        float s_ = (__uint_as_float(ps[0][0]) + __uint_as_float(ps[0][2])) + (__uint_as_float(ps[1][0]) + __uint_as_float(ps[1][2]));
        float q_ = (__uint_as_float(ps[0][1]) + __uint_as_float(ps[0][3])) + (__uint_as_float(ps[1][1]) + __uint_as_float(ps[1][3]));
        s_ = dpp_add<0xB1>(s_); q_ = dpp_add<0xB1>(q_);
        s_ = dpp_add<0x4E>(s_); q_ = dpp_add<0x4E>(q_);
        s_ = dpp_add<0x141>(s_); q_ = dpp_add<0x141>(q_);
        float mb, sd, m, v;
        ln_consts(s_, q_, mb, sd, m, v);
        float M = mb, R = 1.0f / sd;
        if constexpr (MODE == 4) {
          float m2 = (m - mb) * R, v2 = v * R * R;
          float m2b = rbf(m2);
          float sd2 = rbf(__builtin_sqrtf(rbf(rbf(v2) + 1e-5f)));
          M = mb + m2b * sd;
          R = R / sd2;
        }
        if ((tid & 7) == 0) st_lds[tid >> 3] = f32x2{M, R};
      }
    }
  });

  if constexpr (C::LN) {
#pragma unroll
    for (int nt = 0; nt < NT; ++nt) {
      float c = add_x32(add_x16(cs[nt]));
      if (g == 0) cs_lds[wm * 192 + nt * 64 + wn * 16 + l15] = c;
    }
    lds_bar();
  }
  STAMP(MODE, 2);
#pragma unroll
  for (int mt = 0; mt < 2; ++mt) {
    const int lrow = wm * 32 + mt * 16 + l15;
    const int tok = row0 + lrow;
#pragma unroll
    for (int nt = 0; nt < NT; ++nt) {
      f32x4 a = acc[mt][nt];
      if constexpr (C::LN) {
        if (MODE == 1 && nt == 2) {
          float c = cs_lds[nt * 64 + wn * 16 + l15] + cs_lds[192 + nt * 64 + wn * 16 + l15];
#pragma unroll
          for (int i = 0; i < 4; ++i) {
            f32x2 st = st_lds[wm * 32 + mt * 16 + 4 * g + i];
            a[i] = (a[i] - st[0] * c) * st[1];
          }
        } else {
          f32x4 c = *(const f32x4*)(cs_lds + nt * 64 + wn * 16 + 4 * g) + *(const f32x4*)(cs_lds + 192 + nt * 64 + wn * 16 + 4 * g);
          f32x2 st = st_lds[lrow];
#pragma unroll
          for (int i = 0; i < 4; ++i) a[i] = (a[i] - st[0] * c[i]) * st[1];
        }
      }
      if constexpr (MODE == 1) {
        const int head = cb * 4 + wn;
        if (nt < 2) {
          float sc = (nt == 0) ? 0.25f * 1.4426950408889634f : 1.0f;
          u32x2 v = {cvtpk(a[0] * sc, a[1] * sc), cvtpk(a[2] * sc, a[3] * sc)};
          u16* dst = (nt == 0 ? ga.o0 : ga.o1) + ((size_t)(head * 64 + (tok >> 5)) * 2 + (g >> 1)) * 256 + (tok & 31) * 8 + (g & 1) * 4;
          *(u32x2*)dst = v;
        } else {
          const int step = (row0 + wm * 32 + mt * 16) >> 4;
          u32x2 v = {cvtpk(a[0], a[1]), cvtpk(a[2], a[3])};
          u16* dst = ga.o2 + ((size_t)(head * 128 + step) * 2 + (g & 1)) * 128 + l15 * 8 + (g >> 1) * 4;
          *(u32x2*)dst = v;
        }
      } else if constexpr (MODE == 3 || MODE == 5) {
        const int col = cb * 64 + wn * 16 + 4 * g;
        u32x2 r = res0[mt];
        if constexpr (MODE == 5) {
          f32x2 st = st_lds[lrow];
          unsigned d0 = cvtpk(lo2f(r[0]) - st[0], hi2f(r[0]) - st[0]), d1 = cvtpk(lo2f(r[1]) - st[0], hi2f(r[1]) - st[0]);
          r[0] = cvtpk(lo2f(d0) * st[1], hi2f(d0) * st[1]);
          r[1] = cvtpk(lo2f(d1) * st[1], hi2f(d1) * st[1]);
        }
        unsigned a0 = cvtpk(a[0], a[1]), a1 = cvtpk(a[2], a[3]);
        u32x2 v = {cvtpk(lo2f(r[0]) + lo2f(a0), hi2f(r[0]) + hi2f(a0)), cvtpk(lo2f(r[1]) + lo2f(a1), hi2f(r[1]) + hi2f(a1))};
        *(u32x2*)(ga.o0 + (size_t)tok * DM + col) = v;
        float f0 = lo2f(v[0]), f1 = hi2f(v[0]), f2 = lo2f(v[1]), f3 = hi2f(v[1]);
        float s_ = (f0 + f1) + (f2 + f3), q_ = fmaf(f0, f0, f1 * f1) + fmaf(f2, f2, f3 * f3);
        s_ = add_x32(add_x16(s_)); q_ = add_x32(add_x16(q_));
        if (g == 0) *(f32x2*)(ga.pout + (size_t)tok * 64 + (cb * 4 + wn) * 2) = f32x2{s_, q_};
      } else if constexpr (MODE == 4) {
        const int col = cb * 64 + wn * 16 + 4 * g;
        unsigned a0 = cvtpk(a[0], a[1]), a1 = cvtpk(a[2], a[3]);
        u32x2 v = {cvtpk(gelu_tanh(lo2f(a0)), gelu_tanh(hi2f(a0))), cvtpk(gelu_tanh(lo2f(a1)), gelu_tanh(hi2f(a1)))};
        const int grp = cb >> 1;
        const int sh = (grp == 0) ? 1 : (grp == 1) ? -1 : (grp == 2) ? 2 : -2;
        const int dt = tok + sh;
        if (dt >= 0 && dt < SEQ) *(u32x2*)(ga.o0 + (size_t)dt * DM + col) = v;
        const int src = tok - sh;
        if (src < 0 || src >= SEQ) *(u32x2*)(ga.o0 + (size_t)tok * DM + col) = u32x2{0u, 0u};
      } else if constexpr (MODE == 6) {
        const int col = cb * 192 + nt * 64 + wn * 16 + 4 * g;
        unsigned a0 = cvtpk(a[0], a[1]), a1 = cvtpk(a[2], a[3]);
        u32x2 v = {cvtpk(gelu_tanh(lo2f(a0)), gelu_tanh(hi2f(a0))), cvtpk(gelu_tanh(lo2f(a1)), gelu_tanh(hi2f(a1)))};
        if (col < FF) *(u32x2*)(ga.o0 + (size_t)tok * FF + col) = v;
      } else {
        const int col = cb * 64 + wn * 16 + 4 * g;
        u32x2 x1 = res0[mt], hh = res1[mt];
        unsigned a0 = cvtpk(a[0], a[1]), a1 = cvtpk(a[2], a[3]);
        unsigned b0 = cvtpk(lo2f(x1[0]) + lo2f(a0), hi2f(x1[0]) + hi2f(a0));
        unsigned b1 = cvtpk(lo2f(x1[1]) + lo2f(a1), hi2f(x1[1]) + hi2f(a1));
        u32x2 v = {cvtpk(lo2f(hh[0]) + lo2f(b0), hi2f(hh[0]) + hi2f(b0)), cvtpk(lo2f(hh[1]) + lo2f(b1), hi2f(hh[1]) + hi2f(b1))};
        *(u32x2*)(ga.o0 + (size_t)tok * DM + col) = v;
      }
    }
  }
  STAMP(MODE, 3);
}

constexpr int AT_K = 0, AT_V = 65536, AT_ONES = 131072, AT_Q = AT_ONES + 8192, AT_LDS = AT_Q + 8192;

__global__ __launch_bounds__(512, 2) void attn_kernel(const u16* Qf, const u16* Kf, const u16* Vf, u16* O) {
  extern __shared__ __attribute__((aligned(16))) char lds[];
  STAMP(2, 0);
  const int tid = threadIdx.x, lane = tid & 63;
  const int wid = __builtin_amdgcn_readfirstlane(tid >> 6);
  const int xcd = blockIdx.x & 7, idx = blockIdx.x >> 3;
  const int head = xcd * 4 + (idx >> 3), qb = idx & 7;
  const unsigned lds0 = (unsigned)(uintptr_t)lds;
  const int r = lane & 31, h = lane >> 5;
  const char* kg = (const char*)Kf + (size_t)head * 65536;
  const char* vg = (const char*)Vf + (size_t)head * 65536;
  const char* qg = (const char*)Qf + (size_t)head * 65536 + (qb * 8 + wid) * 1024;
  const unsigned lo = lane * 16;

  glds16(qg, lo, lds0 + AT_Q + wid * 1024);
#pragma unroll
  for (int c = 0; c < 8; ++c) {
    glds16(kg, lo + c * 8192 + wid * 1024, lds0 + AT_K + c * 8192 + wid * 1024);
    glds16(vg, lo + c * 8192 + wid * 1024, lds0 + AT_V + c * 8192 + wid * 1024);
  }
  *(u32x4*)(lds + AT_ONES + tid * 16) = u32x4{0x3F803F80u, 0x3F803F80u, 0x3F803F80u, 0x3F803F80u};

  lds_cptr kp = (lds_cptr)lds + AT_K + lane * 16;
  lds_cptr vp = (lds_cptr)lds + ((r < 16) ? (AT_V + h * 256 + r * 16) : (AT_ONES + (r - 16) * 16));
  const int vstep = (r < 16) ? 4096 : 0;

  f32x16 o, negm, sA, sB;
#pragma unroll
  for (int i = 0; i < 16; ++i) { o[i] = 0.f; negm[i] = 0.f; }

  wait_vm_bar<10>();
  STAMP(2, 1);
  const bf16x8 qf = lds_rd128((lds_cptr)lds + AT_Q + wid * 1024 + lane * 16);
  bf16x8 kfA, kfB, vA0, vA1, vB0, vB1;
  u32x4 pA0, pA1, pB0, pB1;
  {
    sA = MFMA32(lds_rd128(kp), qf, negm);
    kfA = lds_rd128(kp + 1024);
    float mx = sA[0];
#pragma unroll
    for (int i = 1; i < 16; ++i) mx = fmaxf(mx, sA[i]);
    const float d = max_x32(mx) + 7.0f;
#pragma unroll
    for (int i = 0; i < 16; ++i) { negm[i] = -d; sA[i] -= d; }
    vB0 = lds_rd128(vp); vB1 = vB0;
    pB0 = u32x4{0u, 0u, 0u, 0u}; pB1 = pB0;
  }

#define SBAR() __builtin_amdgcn_sched_barrier(0)
#define PK(x, i) __builtin_amdgcn_perm(__float_as_uint(x[(i) + 1]), __float_as_uint(x[i]), 0x07060302u)
#define EXPS(lo_, hi_) _Pragma("unroll") for (int i = lo_; i < hi_; ++i) sc[i] = __builtin_amdgcn_exp2f(sc[i])
  auto step = [&](f32x16& sc, f32x16& sn, const bf16x8& kf, bf16x8& kfn, const bf16x8& vp0, const bf16x8& vp1,
                  const u32x4& pp0, const u32x4& pp1, bf16x8& vc0, bf16x8& vc1, u32x4& pc0, u32x4& pc1, lds_cptr kc,
                  lds_cptr vc) {
    sn = MFMA32(kf, qf, negm);
    kfn = lds_rd128(kc + 2048);
    vc0 = lds_rd128(vc); vc1 = lds_rd128(vc + 512);
    SBAR();
    EXPS(0, 5);
    SBAR();
    o = MFMA32(vp0, __builtin_bit_cast(bf16x8, pp0), o);
    SBAR();
    EXPS(5, 11);
    SBAR();
    o = MFMA32(vp1, __builtin_bit_cast(bf16x8, pp1), o);
    SBAR();
    EXPS(11, 16);
    SBAR();
#pragma unroll
    for (int i = 0; i < 4; ++i) { pc0[i] = PK(sc, 2 * i); pc1[i] = PK(sc, 8 + 2 * i); }
    unsigned chk = (pc0[0] | pc0[1] | pc0[2]) | (pc0[3] | pc1[0] | pc1[1]) | (pc1[2] | pc1[3]);
    if (__builtin_expect(__any((chk & 0x40004000u) != 0), 0)) {
      f32x16 s2 = MFMA32(lds_rd128(kc), qf, negm);
      float mx = s2[0];
#pragma unroll
      for (int i = 1; i < 16; ++i) mx = fmaxf(mx, s2[i]);
      const float d = fmaxf(max_x32(mx) + 7.0f, 0.f);
      const float alpha = __builtin_amdgcn_exp2f(-d);
#pragma unroll
      for (int i = 0; i < 16; ++i) { o[i] *= alpha; negm[i] -= d; sn[i] -= d; s2[i] = __builtin_amdgcn_exp2f(s2[i] - d); }
#pragma unroll
      for (int i = 0; i < 4; ++i) { pc0[i] = PK(s2, 2 * i); pc1[i] = PK(s2, 8 + 2 * i); }
    }
    SBAR();
  };
  auto group = [&]() {
    step(sA, sB, kfA, kfB, vB0, vB1, pB0, pB1, vA0, vA1, pA0, pA1, kp, vp);
    step(sB, sA, kfB, kfA, vA0, vA1, pA0, pA1, vB0, vB1, pB0, pB1, kp + 1024, vp + 1024);
    step(sA, sB, kfA, kfB, vB0, vB1, pB0, pB1, vA0, vA1, pA0, pA1, kp + 2048, vp + 2048);
    step(sB, sA, kfB, kfA, vA0, vA1, pA0, pA1, vB0, vB1, pB0, pB1, kp + 3072, vp + 3072);
    kp += 4096;
    vp += vstep;
  };
#pragma unroll 1
  for (int it = 0; it < 4; ++it) group();
  wait_vm_bar<0>();
#pragma unroll 1
  for (int it = 4; it < 16; ++it) group();
  o = MFMA32(vB0, __builtin_bit_cast(bf16x8, pB0), o);
  o = MFMA32(vB1, __builtin_bit_cast(bf16x8, pB1), o);

  STAMP(2, 2);
  const float inv = 1.0f / o[8];
  const int q = qb * 256 + wid * 32 + r;
  u16* dst = O + (size_t)q * DM + head * 16 + 4 * h;
  *(u32x2*)dst = u32x2{cvtpk(o[0] * inv, o[1] * inv), cvtpk(o[2] * inv, o[3] * inv)};
  *(u32x2*)(dst + 8) = u32x2{cvtpk(o[4] * inv, o[5] * inv), cvtpk(o[6] * inv, o[7] * inv)};
  STAMP(2, 3);
}

template <int MODE>
static void launch_gemm(const GArgs& ga, hipStream_t stream) {
  (void)hipFuncSetAttribute((const void*)gemm_kernel<MODE>, hipFuncAttributeMaxDynamicSharedMemorySize, Cfg<MODE>::LDS);
  gemm_kernel<MODE><<<256, 640, Cfg<MODE>::LDS, stream>>>(ga);
}

extern "C" void kernel_launch(void* const* d_in, const int* in_sizes, int n_in, void* d_out, int out_size, void* d_ws,
                              size_t ws_size, hipStream_t stream) {
  const u16* x = (const u16*)d_in[0];
  const u16* wqkv = (const u16*)d_in[1];
  const u16* wo = (const u16*)d_in[2];
  const u16* w1 = (const u16*)d_in[4];
  const u16* w2 = (const u16*)d_in[5];
  const u16* w3 = (const u16*)d_in[6];
  const u16* w4 = (const u16*)d_in[7];
  char* ws = (char*)d_ws;
  u16 *Q = (u16*)(ws + WS_Q), *Kf = (u16*)(ws + WS_K), *Vf = (u16*)(ws + WS_V), *O = (u16*)(ws + WS_O),
      *H = (u16*)(ws + WS_H), *T = (u16*)(ws + WS_T), *X1 = (u16*)(ws + WS_X1), *U = (u16*)(ws + WS_U);
  float *PH = (float*)(ws + WS_PH), *PX = (float*)(ws + WS_PX);
  u16* out = (u16*)d_out;

  launch_gemm<1>(GArgs{x, wqkv, nullptr, nullptr, nullptr, nullptr, Q, Kf, Vf}, stream);
  (void)hipFuncSetAttribute((const void*)attn_kernel, hipFuncAttributeMaxDynamicSharedMemorySize, AT_LDS);
  attn_kernel<<<256, 512, AT_LDS, stream>>>(Q, Kf, Vf, O);
  launch_gemm<3>(GArgs{O, wo, x, nullptr, nullptr, PH, H, nullptr, nullptr}, stream);
  launch_gemm<4>(GArgs{H, w1, nullptr, nullptr, PH, nullptr, T, nullptr, nullptr}, stream);
  launch_gemm<5>(GArgs{T, w2, H, nullptr, PH, PX, X1, nullptr, nullptr}, stream);
  launch_gemm<6>(GArgs{X1, w3, nullptr, nullptr, PX, nullptr, U, nullptr, nullptr}, stream);
  launch_gemm<7>(GArgs{U, w4, X1, H, nullptr, nullptr, out, nullptr, nullptr}, stream);
}
```

```cpp
#include <hip/hip_runtime.h>
#include <hip/hip_bf16.h>
#include <stdint.h>

typedef unsigned short u16;
typedef __attribute__((ext_vector_type(8))) short bf16x8;
typedef __attribute__((ext_vector_type(4))) short s16x4;
typedef __attribute__((ext_vector_type(2))) float f32x2;
typedef __attribute__((ext_vector_type(4))) float f32x4;
typedef __attribute__((ext_vector_type(16))) float f32x16;
typedef __attribute__((ext_vector_type(2))) __bf16 bf16x2_t;
typedef __attribute__((ext_vector_type(4))) unsigned u32x4;
typedef __attribute__((ext_vector_type(2))) unsigned u32x2;
typedef short v4i16_t __attribute__((ext_vector_type(4)));
typedef __attribute__((address_space(3))) const char* lds_cptr;

#define DEV __device__ __forceinline__
#define MFMA16(a, b, c) __builtin_amdgcn_mfma_f32_16x16x32_bf16((a), (b), (c), 0, 0, 0)
#define MFMA32(a, b, c) __builtin_amdgcn_mfma_f32_32x32x16_bf16((a), (b), (c), 0, 0, 0)

constexpr int SEQ = 2048, DM = 512, NH = 32, FF = 1376;

constexpr size_t WS_Q = 0, WS_K = 2u << 20, WS_V = 4u << 20, WS_O = 6u << 20, WS_H = 8u << 20, WS_PH = 10u << 20,
                 WS_PX = 11u << 20, WS_T = 12u << 20, WS_X1 = 14u << 20, WS_U = 16u << 20;

DEV unsigned cvtpk(float lo, float hi) {
  f32x2 v = {lo, hi};
  bf16x2_t b = __builtin_convertvector(v, bf16x2_t);
  return __builtin_bit_cast(unsigned, b);
}
DEV float lo2f(unsigned p) { return __uint_as_float(p << 16); }
DEV float hi2f(unsigned p) { return __uint_as_float(p & 0xffff0000u); }
DEV float rbf(float x) { return lo2f(cvtpk(x, x)); }

DEV void glds16(const void* gbase, unsigned voff, unsigned lds_base) {
  unsigned sv;
  asm volatile("s_mov_b32 %0, m0\n\ts_mov_b32 m0, %3\n\ts_nop 0\n\tglobal_load_lds_dwordx4 %1, %2\n\ts_mov_b32 m0, %0"
               : "=&s"(sv)
               : "v"(voff), "s"(gbase), "s"(lds_base)
               : "memory");
}
DEV u32x4 aload4(const void* gbase, unsigned voff) {
  u32x4 r;
  asm volatile("global_load_dwordx4 %0, %1, %2" : "=v"(r) : "v"(voff), "s"(gbase) : "memory");
  return r;
}
DEV u32x2 aload2(const void* gbase, unsigned voff) {
  u32x2 r;
  asm volatile("global_load_dwordx2 %0, %1, %2" : "=v"(r) : "v"(voff), "s"(gbase) : "memory");
  return r;
}
template <class T>
DEV void pin(T& x) { asm volatile("" : "+v"(x)); }
template <int N>
DEV void wait_vm_bar() {
  asm volatile("s_waitcnt vmcnt(%0) lgkmcnt(0)\n\ts_barrier" ::"i"(N) : "memory");
}
DEV void lds_bar() { asm volatile("s_waitcnt lgkmcnt(0)\n\ts_barrier" ::: "memory"); }
DEV bf16x8 lds_rd128(lds_cptr p) { return *(const __attribute__((address_space(3))) bf16x8*)p; }
DEV s16x4 lds_tr(lds_cptr p) {
  return __builtin_bit_cast(s16x4, __builtin_amdgcn_ds_read_tr16_b64_v4i16((__attribute__((address_space(3))) v4i16_t*)p));
}
template <int CTRL>
DEV float dpp_add(float v) {
  int x = __builtin_amdgcn_update_dpp(0, __float_as_int(v), CTRL, 0xf, 0xf, true);
  return v + __int_as_float(x);
}
DEV float add_x16(float v) {
  auto p = __builtin_amdgcn_permlane16_swap(__float_as_uint(v), __float_as_uint(v), false, false);
  return __uint_as_float(p[0]) + __uint_as_float(p[1]);
}
DEV float add_x32(float v) {
  auto p = __builtin_amdgcn_permlane32_swap(__float_as_uint(v), __float_as_uint(v), false, false);
  return __uint_as_float(p[0]) + __uint_as_float(p[1]);
}
DEV float max_x32(float v) {
  auto p = __builtin_amdgcn_permlane32_swap(__float_as_uint(v), __float_as_uint(v), false, false);
  return fmaxf(__uint_as_float(p[0]), __uint_as_float(p[1]));
}
DEV float wave_sum(float v) {
  v = dpp_add<0x121>(v);
  v = dpp_add<0x122>(v);
  v = dpp_add<0x124>(v);
  v = dpp_add<0x128>(v);
  return add_x32(add_x16(v));
}
DEV float dot2_ones(unsigned p, float acc) {
  return __builtin_amdgcn_fdot2_f32_bf16(__builtin_bit_cast(bf16x2_t, p), __builtin_bit_cast(bf16x2_t, 0x3F803F80u), acc, false);
}
DEV float dot2u(unsigned a, unsigned b, float acc) {
  return __builtin_amdgcn_fdot2_f32_bf16(__builtin_bit_cast(bf16x2_t, a), __builtin_bit_cast(bf16x2_t, b), acc, false);
}
DEV float gelu_tanh(float x) {
  const float c = -2.0f * 0.7978845608028654f * 1.4426950408889634f;
  float u = x * (1.0f + 0.044715f * x * x);
  float e = __builtin_amdgcn_exp2f(c * u);
  return x * __builtin_amdgcn_rcpf(1.0f + e);
}
DEV int bswz(int k) { return ((k >> 1) & 1) | (((k >> 3) & 1) << 1); }

#define STAMP(kid, i) do { } while (0)
#define DBG(kid, w, i) do { } while (0)

template <int I, int N, class F>
DEV void static_for(F&& f) {
  if constexpr (I < N) {
    f(std::integral_constant<int, I>{});
    static_for<I + 1, N>(f);
  }
}

DEV void ln_consts(float s, float q, float& mb, float& sd, float& m, float& v) {
  m = s * (1.0f / DM);
  v = fmaxf(q * (1.0f / DM) - m * m, 0.f);
  mb = rbf(m);
  sd = rbf(__builtin_sqrtf(rbf(rbf(v) + 1e-5f)));
}

constexpr int NLD = 4;
template <int MODE>
struct Cfg {
  static constexpr int NT = (MODE == 1 || MODE == 6) ? 3 : 1;
  static constexpr bool LN = (MODE == 1 || MODE == 4 || MODE == 6);
  static constexpr bool PST = (MODE == 4 || MODE == 5 || MODE == 6);
  static constexpr int K = (MODE == 7) ? FF : DM;
  static constexpr int LDA = K;
  static constexpr int LDW = (MODE == 1) ? 3 * DM : (MODE == 6 ? FF : DM);
  static constexpr int NSTAGE = (K + 63) / 64;
  static constexpr int SPB = (NT == 1) ? 2 : 1;
  static constexpr int NB = (NSTAGE + SPB - 1) / SPB;
  static constexpr int RS = 128 * NT;
  static constexpr int BBYTES = 64 * RS;
  static constexpr int SLOT = BBYTES + 8192;
  static constexpr int NGSLOT = 4;
  static constexpr int NSLOT = NGSLOT * SPB;
  static constexpr int DEPTH = NGSLOT - 1;
  static constexpr int X_ST = NSLOT * SLOT;
  static constexpr int X_CS = X_ST + 512;
  static constexpr int LDS = X_CS + 1536;
  static constexpr int LPS = 8 * (NT + 1) / NLD;
};

struct GArgs {
  const u16* A;
  const u16* W;
  const u16* r0;
  const u16* r1;
  const float* pin_;
  float* pout;
  u16* o0;
  u16* o1;
  u16* o2;
};

template <int MODE>
__global__ __launch_bounds__(512 + 64 * NLD, 2) void gemm_kernel(GArgs ga) {
  using C = Cfg<MODE>;
  constexpr int NT = C::NT;
  extern __shared__ __attribute__((aligned(16))) char lds[];
  STAMP(MODE, 0);
  const int tid = threadIdx.x, lane = tid & 63;
  const int wid = __builtin_amdgcn_readfirstlane(tid >> 6);
  const int xcd = blockIdx.x & 7, idx = blockIdx.x >> 3;
  const int rb = xcd * 4 + (idx >> 3), cb = idx & 7;
  const int row0 = rb * 64;
  const unsigned lds0 = (unsigned)(uintptr_t)lds;

  if (wid >= 8) {
    const int L = wid - 8;
    constexpr int IA = 8 / NLD, IB = 8 * NT / NLD;
    unsigned boff[IB];
    int bk[IB];
#pragma unroll
    for (int j = 0; j < IB; ++j) {
      int o = (L * IB + j) * 1024 + lane * 16;
      int k = o / C::RS, rem = o % C::RS;
      int grp = rem >> 7, blk = (rem >> 5) & 3, hf = (rem >> 4) & 1;
      int col;
      if (MODE == 1) col = grp * 512 + cb * 64;
      else col = cb * (64 * NT) + grp * 64;
      col += ((blk ^ bswz(k)) * 16) + hf * 8;
      if (MODE == 6 && col > FF - 8) col = FF - 8;
      bk[j] = k;
      boff[j] = (unsigned)(k * C::LDW + col) * 2u;
    }
    unsigned aoffj[IA];
#pragma unroll
    for (int j = 0; j < IA; ++j) {
      int r = (L * IA + j) * 8 + (lane >> 3), c = (lane & 7) ^ ((r >> 1) & 7);
      aoffj[j] = (unsigned)((row0 + r) * C::LDA + c * 8) * 2u;
    }
    auto issue_sub = [&](int s) {
      const unsigned slot = lds0 + (unsigned)((s % C::NSLOT) * C::SLOT);
#pragma unroll
      for (int j = 0; j < IA; ++j) glds16(ga.A, aoffj[j] + s * 128, slot + C::BBYTES + (L * IA + j) * 1024);
#pragma unroll
      for (int j = 0; j < IB; ++j) {
        unsigned off = boff[j] + (unsigned)(s * 64 * C::LDW) * 2u;
        if (MODE == 7 && s == C::NSTAGE - 1 && s * 64 + bk[j] >= FF) off = boff[j] + (unsigned)((FF - 64) * C::LDW) * 2u;
        glds16(ga.W, off, slot + (unsigned)(L * IB + j) * 1024u);
      }
    };
    auto issue_group = [&](auto g_) {
      constexpr int gg = g_.value;
      static_for<0, C::SPB>([&](auto u_) { if constexpr (gg < C::NB && gg * C::SPB + u_.value < C::NSTAGE) issue_sub(gg * C::SPB + u_.value); });
    };
    issue_group(std::integral_constant<int, 0>{});
    issue_group(std::integral_constant<int, 1>{});
    static_for<0, C::NB>([&](auto b_) {
      constexpr int b = b_.value;
      constexpr int lastg0 = (b == 0) ? 1 : b + C::DEPTH - 1;
      constexpr int lastg = lastg0 < C::NB - 1 ? lastg0 : C::NB - 1;
      constexpr int sub_after = ((lastg + 1) * C::SPB > C::NSTAGE ? C::NSTAGE : (lastg + 1) * C::SPB) - ((b + 1) * C::SPB > C::NSTAGE ? C::NSTAGE : (b + 1) * C::SPB);
      asm volatile("s_waitcnt vmcnt(%0)" ::"i"(sub_after * C::LPS) : "memory");
      if (wid == 8) DBG(MODE, 1, b);
      wait_vm_bar<sub_after * C::LPS>();
      if constexpr (b == 0) {
        static_for<2, C::DEPTH + 1>([&](auto g_) { issue_group(g_); });
      } else {
        issue_group(std::integral_constant<int, b + C::DEPTH>{});
      }
    });
    return;
  }

  const int wm = wid & 1, wn = wid >> 1;
  const int l15 = lane & 15, g = lane >> 4;
  u32x4 ps[2];
  u32x2 res0[2], res1[2];
  if constexpr (C::PST) {
    const float* pp = ga.pin_ + (size_t)(row0 + (tid >> 3)) * 64 + (tid & 7) * 8;
    ps[0] = *(const u32x4*)pp;
    ps[1] = *(const u32x4*)(pp + 4);
  }
  if constexpr (MODE == 3 || MODE == 5 || MODE == 7) {
#pragma unroll
    for (int mt = 0; mt < 2; ++mt) {
      size_t o = (size_t)(row0 + wm * 32 + mt * 16 + l15) * DM + cb * 64 + wn * 16 + 4 * g;
      res0[mt] = *(const u32x2*)(ga.r0 + o);
      if constexpr (MODE == 7) res1[mt] = *(const u32x2*)(ga.r1 + o);
    }
  }

  int xoff[2][2];
#pragma unroll
  for (int mt = 0; mt < 2; ++mt)
#pragma unroll
    for (int kk = 0; kk < 2; ++kk) {
      int r = wm * 32 + mt * 16 + l15;
      xoff[mt][kk] = C::BBYTES + r * 128 + (((4 * kk + g) ^ ((r >> 1) & 7)) * 16);
    }
  int woff[2][2];
  {
    int q = l15 >> 2, p = l15 & 3;
#pragma unroll
    for (int kk = 0; kk < 2; ++kk)
#pragma unroll
      for (int t = 0; t < 2; ++t) {
        int k = 32 * kk + 8 * g + 4 * t + q;
        woff[kk][t] = k * C::RS + ((wn ^ bswz(k)) * 32) + p * 8;
      }
  }

  f32x4 acc[2][NT];
  float cs[NT];
#pragma unroll
  for (int nt = 0; nt < NT; ++nt) {
    cs[nt] = 0.f;
#pragma unroll
    for (int mt = 0; mt < 2; ++mt) acc[mt][nt] = f32x4{0.f, 0.f, 0.f, 0.f};
  }
  f32x2* st_lds = (f32x2*)(lds + C::X_ST);
  float* cs_lds = (float*)(lds + C::X_CS);

    {
      if constexpr (C::PST) {
        float s_ = (__uint_as_float(ps[0][0]) + __uint_as_float(ps[0][2])) + (__uint_as_float(ps[1][0]) + __uint_as_float(ps[1][2]));
        float q_ = (__uint_as_float(ps[0][1]) + __uint_as_float(ps[0][3])) + (__uint_as_float(ps[1][1]) + __uint_as_float(ps[1][3]));
        s_ = dpp_add<0xB1>(s_); q_ = dpp_add<0xB1>(q_);
        s_ = dpp_add<0x4E>(s_); q_ = dpp_add<0x4E>(q_);
        s_ = dpp_add<0x141>(s_); q_ = dpp_add<0x141>(q_);
        float mb, sd, m, v;
        ln_consts(s_, q_, mb, sd, m, v);
        float M = mb, R = 1.0f / sd;
        if constexpr (MODE == 4) {
          float m2 = (m - mb) * R, v2 = v * R * R;
          float m2b = rbf(m2);
          float sd2 = rbf(__builtin_sqrtf(rbf(rbf(v2) + 1e-5f)));
          M = mb + m2b * sd;
          R = R / sd2;
        }
        if ((tid & 7) == 0) st_lds[tid >> 3] = f32x2{M, R};
      }
    }
  float sx[2] = {0.f, 0.f}, qx[2] = {0.f, 0.f};
  const unsigned sel0 = (wm == 0) ? 0x3F803F80u : 0u, sel1 = (wm == 1) ? 0x3F803F80u : 0u;
  static_for<0, C::NB>([&](auto b_) {
    constexpr int s = b_.value;
    if (wid == 0) DBG(MODE, 2, s);
    lds_bar();
    if (wid == 0) DBG(MODE, 0, s);
    if constexpr (s == 0) STAMP(MODE, 1);
    constexpr int S0 = s * C::SPB;
    constexpr int NSUB = ((S0 + C::SPB > C::NSTAGE) ? C::NSTAGE : S0 + C::SPB) - S0;
    constexpr int NKS = 2 * NSUB - ((MODE == 7 && S0 + NSUB == C::NSTAGE) ? 1 : 0);
    bf16x8 xf[NKS][2], wf[NKS][NT];
#pragma unroll
    for (int ks = 0; ks < NKS; ++ks) {
      const lds_cptr bs = (lds_cptr)lds + ((S0 + (ks >> 1)) % C::NSLOT) * C::SLOT;
      const int kk = ks & 1;
#pragma unroll
      for (int nt = 0; nt < NT; ++nt) {
        s16x4 w0 = lds_tr(bs + woff[kk][0] + nt * 128);
        s16x4 w1 = lds_tr(bs + woff[kk][1] + nt * 128);
        wf[ks][nt] = __builtin_shufflevector(w0, w1, 0, 1, 2, 3, 4, 5, 6, 7);
        if (nt == 0) {
#pragma unroll
          for (int mt = 0; mt < 2; ++mt) xf[ks][mt] = lds_rd128(bs + xoff[mt][kk]);
        }
      }
    }
#pragma unroll
    for (int ks = 0; ks < NKS; ++ks) {
#pragma unroll
      for (int nt = 0; nt < NT; ++nt) {
#pragma unroll
        for (int mt = 0; mt < 2; ++mt) {
          if (MODE == 1 && nt == 2) acc[mt][nt] = MFMA16(xf[ks][mt], wf[ks][nt], acc[mt][nt]);
          else acc[mt][nt] = MFMA16(wf[ks][nt], xf[ks][mt], acc[mt][nt]);
        }
        if constexpr (C::LN) {
          u32x4 wu = __builtin_bit_cast(u32x4, wf[ks][nt]);
          const unsigned sel = (ks & 1) ? sel1 : sel0;
#pragma unroll
          for (int e = 0; e < 4; ++e) cs[nt] = dot2u(wu[e], sel, cs[nt]);
        }
      }
    }
    if constexpr (MODE == 1) {
      auto stat = [&](const bf16x8 (&xx)[2]) {
#pragma unroll
        for (int mt = 0; mt < 2; ++mt) {
          u32x4 xu = __builtin_bit_cast(u32x4, xx[mt]);
#pragma unroll
          for (int e = 0; e < 4; ++e) {
            sx[mt] = dot2_ones(xu[e], sx[mt]);
            qx[mt] = dot2u(xu[e], xu[e], qx[mt]);
          }
        }
      };
      if (wn == ((2 * s) & 3)) stat(xf[0]);
      else if (wn == ((2 * s + 1) & 3)) stat(xf[1]);
    }
    if constexpr (C::LN) {
#pragma unroll
      for (int nt = 0; nt < NT; ++nt) pin(cs[nt]);
    }
    if constexpr (MODE == 1) { pin(sx[0]); pin(sx[1]); pin(qx[0]); pin(qx[1]); }
    __builtin_amdgcn_sched_barrier(0);
  });

  if constexpr (C::LN) {
#pragma unroll
    for (int nt = 0; nt < NT; ++nt) {
      float c = add_x32(add_x16(cs[nt]));
      if (g == 0) cs_lds[wm * 192 + nt * 64 + wn * 16 + l15] = c;
    }
    if constexpr (MODE == 1) {
      f32x2* px = (f32x2*)lds;
      float a0 = add_x32(add_x16(sx[0])), b0 = add_x32(add_x16(qx[0])), a1 = add_x32(add_x16(sx[1])), b1 = add_x32(add_x16(qx[1]));
      lds_bar();
      if (g == 0) { px[wn * 64 + wm * 32 + l15] = f32x2{a0, b0}; px[wn * 64 + wm * 32 + 16 + l15] = f32x2{a1, b1}; }
      lds_bar();
      if (tid < 64) {
        f32x2 t = (px[tid] + px[64 + tid]) + (px[128 + tid] + px[192 + tid]);
        float mb, sd, m, v;
        ln_consts(t[0], t[1], mb, sd, m, v);
        st_lds[tid] = f32x2{mb, 1.0f / sd};
      }
    }
    lds_bar();
  }
  STAMP(MODE, 2);
#pragma unroll
  for (int mt = 0; mt < 2; ++mt) {
    const int lrow = wm * 32 + mt * 16 + l15;
    const int tok = row0 + lrow;
#pragma unroll
    for (int nt = 0; nt < NT; ++nt) {
      f32x4 a = acc[mt][nt];
      if constexpr (C::LN) {
        if (MODE == 1 && nt == 2) {
          float c = cs_lds[nt * 64 + wn * 16 + l15] + cs_lds[192 + nt * 64 + wn * 16 + l15];
#pragma unroll
          for (int i = 0; i < 4; ++i) {
            f32x2 st = st_lds[wm * 32 + mt * 16 + 4 * g + i];
            a[i] = (a[i] - st[0] * c) * st[1];
          }
        } else {
          f32x4 c = *(const f32x4*)(cs_lds + nt * 64 + wn * 16 + 4 * g) + *(const f32x4*)(cs_lds + 192 + nt * 64 + wn * 16 + 4 * g);
          f32x2 st = st_lds[lrow];
#pragma unroll
          for (int i = 0; i < 4; ++i) a[i] = (a[i] - st[0] * c[i]) * st[1];
        }
      }
      if constexpr (MODE == 1) {
        const int head = cb * 4 + wn;
        if (nt < 2) {
          float sc = (nt == 0) ? 0.25f * 1.4426950408889634f : 1.0f;
          u32x2 v = {cvtpk(a[0] * sc, a[1] * sc), cvtpk(a[2] * sc, a[3] * sc)};
          u16* dst = (nt == 0 ? ga.o0 : ga.o1) + ((size_t)(head * 64 + (tok >> 5)) * 2 + (g >> 1)) * 256 + (tok & 31) * 8 + (g & 1) * 4;
          *(u32x2*)dst = v;
        } else {
          const int step = (row0 + wm * 32 + mt * 16) >> 4;
          u32x2 v = {cvtpk(a[0], a[1]), cvtpk(a[2], a[3])};
          u16* dst = ga.o2 + ((size_t)(head * 128 + step) * 2 + (g & 1)) * 128 + l15 * 8 + (g >> 1) * 4;
          *(u32x2*)dst = v;
        }
      } else if constexpr (MODE == 3 || MODE == 5) {
        const int col = cb * 64 + wn * 16 + 4 * g;
        u32x2 r = res0[mt];
        if constexpr (MODE == 5) {
          f32x2 st = st_lds[lrow];
          unsigned d0 = cvtpk(lo2f(r[0]) - st[0], hi2f(r[0]) - st[0]), d1 = cvtpk(lo2f(r[1]) - st[0], hi2f(r[1]) - st[0]);
          r[0] = cvtpk(lo2f(d0) * st[1], hi2f(d0) * st[1]);
          r[1] = cvtpk(lo2f(d1) * st[1], hi2f(d1) * st[1]);
        }
        unsigned a0 = cvtpk(a[0], a[1]), a1 = cvtpk(a[2], a[3]);
        u32x2 v = {cvtpk(lo2f(r[0]) + lo2f(a0), hi2f(r[0]) + hi2f(a0)), cvtpk(lo2f(r[1]) + lo2f(a1), hi2f(r[1]) + hi2f(a1))};
        *(u32x2*)(ga.o0 + (size_t)tok * DM + col) = v;
        float f0 = lo2f(v[0]), f1 = hi2f(v[0]), f2 = lo2f(v[1]), f3 = hi2f(v[1]);
        float s_ = (f0 + f1) + (f2 + f3), q_ = fmaf(f0, f0, f1 * f1) + fmaf(f2, f2, f3 * f3);
        s_ = add_x32(add_x16(s_)); q_ = add_x32(add_x16(q_));
        if (g == 0) *(f32x2*)(ga.pout + (size_t)tok * 64 + (cb * 4 + wn) * 2) = f32x2{s_, q_};
      } else if constexpr (MODE == 4) {
        const int col = cb * 64 + wn * 16 + 4 * g;
        unsigned a0 = cvtpk(a[0], a[1]), a1 = cvtpk(a[2], a[3]);
        u32x2 v = {cvtpk(gelu_tanh(lo2f(a0)), gelu_tanh(hi2f(a0))), cvtpk(gelu_tanh(lo2f(a1)), gelu_tanh(hi2f(a1)))};
        const int grp = cb >> 1;
        const int sh = (grp == 0) ? 1 : (grp == 1) ? -1 : (grp == 2) ? 2 : -2;
        const int dt = tok + sh;
        if (dt >= 0 && dt < SEQ) *(u32x2*)(ga.o0 + (size_t)dt * DM + col) = v;
        const int src = tok - sh;
        if (src < 0 || src >= SEQ) *(u32x2*)(ga.o0 + (size_t)tok * DM + col) = u32x2{0u, 0u};
      } else if constexpr (MODE == 6) {
        const int col = cb * 192 + nt * 64 + wn * 16 + 4 * g;
        unsigned a0 = cvtpk(a[0], a[1]), a1 = cvtpk(a[2], a[3]);
        u32x2 v = {cvtpk(gelu_tanh(lo2f(a0)), gelu_tanh(hi2f(a0))), cvtpk(gelu_tanh(lo2f(a1)), gelu_tanh(hi2f(a1)))};
        if (col < FF) *(u32x2*)(ga.o0 + (size_t)tok * FF + col) = v;
      } else {
        const int col = cb * 64 + wn * 16 + 4 * g;
        u32x2 x1 = res0[mt], hh = res1[mt];
        unsigned a0 = cvtpk(a[0], a[1]), a1 = cvtpk(a[2], a[3]);
        unsigned b0 = cvtpk(lo2f(x1[0]) + lo2f(a0), hi2f(x1[0]) + hi2f(a0));
        unsigned b1 = cvtpk(lo2f(x1[1]) + lo2f(a1), hi2f(x1[1]) + hi2f(a1));
        u32x2 v = {cvtpk(lo2f(hh[0]) + lo2f(b0), hi2f(hh[0]) + hi2f(b0)), cvtpk(lo2f(hh[1]) + lo2f(b1), hi2f(hh[1]) + hi2f(b1))};
        *(u32x2*)(ga.o0 + (size_t)tok * DM + col) = v;
      }
    }
  }
  STAMP(MODE, 3);
}

constexpr int AT_K = 0, AT_V = 65536, AT_ONES = 131072, AT_Q = AT_ONES + 8192, AT_LDS = AT_Q + 8192;

__global__ __launch_bounds__(512, 2) void attn_kernel(const u16* Qf, const u16* Kf, const u16* Vf, u16* O) {
  extern __shared__ __attribute__((aligned(16))) char lds[];
  STAMP(2, 0);
  const int tid = threadIdx.x, lane = tid & 63;
  const int wid = __builtin_amdgcn_readfirstlane(tid >> 6);
  const int xcd = blockIdx.x & 7, idx = blockIdx.x >> 3;
  const int head = xcd * 4 + (idx >> 3), qb = idx & 7;
  const unsigned lds0 = (unsigned)(uintptr_t)lds;
  const int r = lane & 31, h = lane >> 5;
  const char* kg = (const char*)Kf + (size_t)head * 65536;
  const char* vg = (const char*)Vf + (size_t)head * 65536;
  const char* qg = (const char*)Qf + (size_t)head * 65536 + (qb * 8 + wid) * 1024;
  const unsigned lo = lane * 16;

  glds16(qg, lo, lds0 + AT_Q + wid * 1024);
#pragma unroll
  for (int c = 0; c < 8; ++c) {
    glds16(kg, lo + c * 8192 + wid * 1024, lds0 + AT_K + c * 8192 + wid * 1024);
    glds16(vg, lo + c * 8192 + wid * 1024, lds0 + AT_V + c * 8192 + wid * 1024);
  }
  *(u32x4*)(lds + AT_ONES + tid * 16) = u32x4{0x3F803F80u, 0x3F803F80u, 0x3F803F80u, 0x3F803F80u};

  lds_cptr kp = (lds_cptr)lds + AT_K + lane * 16;
  lds_cptr vp = (lds_cptr)lds + ((r < 16) ? (AT_V + h * 256 + r * 16) : (AT_ONES + (r - 16) * 16));
  const int vstep = (r < 16) ? 4096 : 0;

  f32x16 o, negm, sA, sB;
#pragma unroll
  for (int i = 0; i < 16; ++i) { o[i] = 0.f; negm[i] = 0.f; }

  wait_vm_bar<10>();
  STAMP(2, 1);
  const bf16x8 qf = lds_rd128((lds_cptr)lds + AT_Q + wid * 1024 + lane * 16);
  bf16x8 kfA, kfB, vA0, vA1, vB0, vB1;
  u32x4 pA0, pA1, pB0, pB1;
  {
    sA = MFMA32(lds_rd128(kp), qf, negm);
    kfA = lds_rd128(kp + 1024);
    float mx = sA[0];
#pragma unroll
    for (int i = 1; i < 16; ++i) mx = fmaxf(mx, sA[i]);
    const float d = max_x32(mx) + 7.0f;
#pragma unroll
    for (int i = 0; i < 16; ++i) { negm[i] = -d; sA[i] -= d; }
    vB0 = lds_rd128(vp); vB1 = vB0;
    pB0 = u32x4{0u, 0u, 0u, 0u}; pB1 = pB0;
  }

#define SBAR() __builtin_amdgcn_sched_barrier(0)
#define PK(x, i) __builtin_amdgcn_perm(__float_as_uint(x[(i) + 1]), __float_as_uint(x[i]), 0x07060302u)
#define EXPS(lo_, hi_) _Pragma("unroll") for (int i = lo_; i < hi_; ++i) sc[i] = __builtin_amdgcn_exp2f(sc[i])
  auto step = [&](f32x16& sc, f32x16& sn, const bf16x8& kf, bf16x8& kfn, const bf16x8& vp0, const bf16x8& vp1,
                  const u32x4& pp0, const u32x4& pp1, bf16x8& vc0, bf16x8& vc1, u32x4& pc0, u32x4& pc1, lds_cptr kc,
                  lds_cptr vc) {
    sn = MFMA32(kf, qf, negm);
    kfn = lds_rd128(kc + 2048);
    vc0 = lds_rd128(vc); vc1 = lds_rd128(vc + 512);
    SBAR();
    EXPS(0, 5);
    SBAR();
    o = MFMA32(vp0, __builtin_bit_cast(bf16x8, pp0), o);
    SBAR();
    EXPS(5, 11);
    SBAR();
    o = MFMA32(vp1, __builtin_bit_cast(bf16x8, pp1), o);
    SBAR();
    EXPS(11, 16);
    SBAR();
#pragma unroll
    for (int i = 0; i < 4; ++i) { pc0[i] = PK(sc, 2 * i); pc1[i] = PK(sc, 8 + 2 * i); }
    unsigned chk = (pc0[0] | pc0[1] | pc0[2]) | (pc0[3] | pc1[0] | pc1[1]) | (pc1[2] | pc1[3]);
    if (__builtin_expect(__any((chk & 0x40004000u) != 0), 0)) {
      f32x16 s2 = MFMA32(lds_rd128(kc), qf, negm);
      float mx = s2[0];
#pragma unroll
      for (int i = 1; i < 16; ++i) mx = fmaxf(mx, s2[i]);
      const float d = fmaxf(max_x32(mx) + 7.0f, 0.f);
      const float alpha = __builtin_amdgcn_exp2f(-d);
#pragma unroll
      for (int i = 0; i < 16; ++i) { o[i] *= alpha; negm[i] -= d; sn[i] -= d; s2[i] = __builtin_amdgcn_exp2f(s2[i] - d); }
#pragma unroll
      for (int i = 0; i < 4; ++i) { pc0[i] = PK(s2, 2 * i); pc1[i] = PK(s2, 8 + 2 * i); }
    }
    SBAR();
  };
  auto group = [&]() {
    step(sA, sB, kfA, kfB, vB0, vB1, pB0, pB1, vA0, vA1, pA0, pA1, kp, vp);
    step(sB, sA, kfB, kfA, vA0, vA1, pA0, pA1, vB0, vB1, pB0, pB1, kp + 1024, vp + 1024);
    step(sA, sB, kfA, kfB, vB0, vB1, pB0, pB1, vA0, vA1, pA0, pA1, kp + 2048, vp + 2048);
    step(sB, sA, kfB, kfA, vA0, vA1, pA0, pA1, vB0, vB1, pB0, pB1, kp + 3072, vp + 3072);
    kp += 4096;
    vp += vstep;
  };
#pragma unroll 1
  for (int it = 0; it < 4; ++it) group();
  wait_vm_bar<0>();
#pragma unroll 1
  for (int it = 4; it < 16; ++it) group();
  o = MFMA32(vB0, __builtin_bit_cast(bf16x8, pB0), o);
  o = MFMA32(vB1, __builtin_bit_cast(bf16x8, pB1), o);

  STAMP(2, 2);
  const float inv = 1.0f / o[8];
  const int q = qb * 256 + wid * 32 + r;
  u16* dst = O + (size_t)q * DM + head * 16 + 4 * h;
  *(u32x2*)dst = u32x2{cvtpk(o[0] * inv, o[1] * inv), cvtpk(o[2] * inv, o[3] * inv)};
  *(u32x2*)(dst + 8) = u32x2{cvtpk(o[4] * inv, o[5] * inv), cvtpk(o[6] * inv, o[7] * inv)};
  STAMP(2, 3);
}

template <int MODE>
static void launch_gemm(const GArgs& ga, hipStream_t stream) {
  (void)hipFuncSetAttribute((const void*)gemm_kernel<MODE>, hipFuncAttributeMaxDynamicSharedMemorySize, Cfg<MODE>::LDS);
  gemm_kernel<MODE><<<256, 512 + 64 * NLD, Cfg<MODE>::LDS, stream>>>(ga);
}

extern "C" void kernel_launch(void* const* d_in, const int* in_sizes, int n_in, void* d_out, int out_size, void* d_ws,
                              size_t ws_size, hipStream_t stream) {
  const u16* x = (const u16*)d_in[0];
  const u16* wqkv = (const u16*)d_in[1];
  const u16* wo = (const u16*)d_in[2];
  const u16* w1 = (const u16*)d_in[4];
  const u16* w2 = (const u16*)d_in[5];
  const u16* w3 = (const u16*)d_in[6];
  const u16* w4 = (const u16*)d_in[7];
  char* ws = (char*)d_ws;
  u16 *Q = (u16*)(ws + WS_Q), *Kf = (u16*)(ws + WS_K), *Vf = (u16*)(ws + WS_V), *O = (u16*)(ws + WS_O),
      *H = (u16*)(ws + WS_H), *T = (u16*)(ws + WS_T), *X1 = (u16*)(ws + WS_X1), *U = (u16*)(ws + WS_U);
  float *PH = (float*)(ws + WS_PH), *PX = (float*)(ws + WS_PX);
  u16* out = (u16*)d_out;

  launch_gemm<1>(GArgs{x, wqkv, nullptr, nullptr, nullptr, nullptr, Q, Kf, Vf}, stream);
  (void)hipFuncSetAttribute((const void*)attn_kernel, hipFuncAttributeMaxDynamicSharedMemorySize, AT_LDS);
  attn_kernel<<<256, 512, AT_LDS, stream>>>(Q, Kf, Vf, O);
  launch_gemm<3>(GArgs{O, wo, x, nullptr, nullptr, PH, H, nullptr, nullptr}, stream);
  launch_gemm<4>(GArgs{H, w1, nullptr, nullptr, PH, nullptr, T, nullptr, nullptr}, stream);
  launch_gemm<5>(GArgs{T, w2, H, nullptr, PH, PX, X1, nullptr, nullptr}, stream);
  launch_gemm<6>(GArgs{X1, w3, nullptr, nullptr, PX, nullptr, U, nullptr, nullptr}, stream);
  launch_gemm<7>(GArgs{U, w4, X1, H, nullptr, nullptr, out, nullptr, nullptr}, stream);
}
```

```cpp
#include <hip/hip_runtime.h>
#include <hip/hip_bf16.h>
#include <stdint.h>

typedef unsigned short u16;
typedef __attribute__((ext_vector_type(8))) short bf16x8;
typedef __attribute__((ext_vector_type(4))) short s16x4;
typedef __attribute__((ext_vector_type(2))) float f32x2;
typedef __attribute__((ext_vector_type(4))) float f32x4;
typedef __attribute__((ext_vector_type(16))) float f32x16;
typedef __attribute__((ext_vector_type(2))) __bf16 bf16x2_t;
typedef __attribute__((ext_vector_type(4))) unsigned u32x4;
typedef __attribute__((ext_vector_type(2))) unsigned u32x2;
typedef short v4i16_t __attribute__((ext_vector_type(4)));
typedef __attribute__((address_space(3))) const char* lds_cptr;

#define DEV __device__ __forceinline__
#define MFMA16(a, b, c) __builtin_amdgcn_mfma_f32_16x16x32_bf16((a), (b), (c), 0, 0, 0)
#define MFMA32(a, b, c) __builtin_amdgcn_mfma_f32_32x32x16_bf16((a), (b), (c), 0, 0, 0)

constexpr int SEQ = 2048, DM = 512, NH = 32, FF = 1376;

constexpr size_t WS_Q = 0, WS_K = 2u << 20, WS_V = 4u << 20, WS_O = 6u << 20, WS_H = 8u << 20, WS_PH = 10u << 20,
                 WS_PX = 11u << 20, WS_T = 12u << 20, WS_X1 = 14u << 20, WS_U = 16u << 20;

DEV unsigned cvtpk(float lo, float hi) {
  f32x2 v = {lo, hi};
  bf16x2_t b = __builtin_convertvector(v, bf16x2_t);
  return __builtin_bit_cast(unsigned, b);
}
DEV float lo2f(unsigned p) { return __uint_as_float(p << 16); }
DEV float hi2f(unsigned p) { return __uint_as_float(p & 0xffff0000u); }
DEV float rbf(float x) { return lo2f(cvtpk(x, x)); }

DEV void glds16(const void* gbase, unsigned voff, unsigned lds_base) {
  unsigned sv;
  asm volatile("s_mov_b32 %0, m0\n\ts_mov_b32 m0, %3\n\ts_nop 0\n\tglobal_load_lds_dwordx4 %1, %2\n\ts_mov_b32 m0, %0"
               : "=&s"(sv)
               : "v"(voff), "s"(gbase), "s"(lds_base)
               : "memory");
}
DEV u32x4 aload4(const void* gbase, unsigned voff) {
  u32x4 r;
  asm volatile("global_load_dwordx4 %0, %1, %2" : "=v"(r) : "v"(voff), "s"(gbase) : "memory");
  return r;
}
DEV u32x2 aload2(const void* gbase, unsigned voff) {
  u32x2 r;
  asm volatile("global_load_dwordx2 %0, %1, %2" : "=v"(r) : "v"(voff), "s"(gbase) : "memory");
  return r;
}
template <class T>
DEV void pin(T& x) { asm volatile("" : "+v"(x)); }
template <int N>
DEV void wait_vm_bar() {
  asm volatile("s_waitcnt vmcnt(%0) lgkmcnt(0)\n\ts_barrier" ::"i"(N) : "memory");
}
DEV void lds_bar() { asm volatile("s_waitcnt lgkmcnt(0)\n\ts_barrier" ::: "memory"); }
DEV bf16x8 lds_rd128(lds_cptr p) { return *(const __attribute__((address_space(3))) bf16x8*)p; }
DEV s16x4 lds_tr(lds_cptr p) {
  return __builtin_bit_cast(s16x4, __builtin_amdgcn_ds_read_tr16_b64_v4i16((__attribute__((address_space(3))) v4i16_t*)p));
}
template <int CTRL>
DEV float dpp_add(float v) {
  int x = __builtin_amdgcn_update_dpp(0, __float_as_int(v), CTRL, 0xf, 0xf, true);
  return v + __int_as_float(x);
}
DEV float add_x16(float v) {
  auto p = __builtin_amdgcn_permlane16_swap(__float_as_uint(v), __float_as_uint(v), false, false);
  return __uint_as_float(p[0]) + __uint_as_float(p[1]);
}
DEV float add_x32(float v) {
  auto p = __builtin_amdgcn_permlane32_swap(__float_as_uint(v), __float_as_uint(v), false, false);
  return __uint_as_float(p[0]) + __uint_as_float(p[1]);
}
DEV float max_x32(float v) {
  auto p = __builtin_amdgcn_permlane32_swap(__float_as_uint(v), __float_as_uint(v), false, false);
  return fmaxf(__uint_as_float(p[0]), __uint_as_float(p[1]));
}
DEV float wave_sum(float v) {
  v = dpp_add<0x121>(v);
  v = dpp_add<0x122>(v);
  v = dpp_add<0x124>(v);
  v = dpp_add<0x128>(v);
  return add_x32(add_x16(v));
}
DEV float dot2_ones(unsigned p, float acc) {
  return __builtin_amdgcn_fdot2_f32_bf16(__builtin_bit_cast(bf16x2_t, p), __builtin_bit_cast(bf16x2_t, 0x3F803F80u), acc, false);
}
DEV float dot2u(unsigned a, unsigned b, float acc) {
  return __builtin_amdgcn_fdot2_f32_bf16(__builtin_bit_cast(bf16x2_t, a), __builtin_bit_cast(bf16x2_t, b), acc, false);
}
DEV float gelu_tanh(float x) {
  const float c = -2.0f * 0.7978845608028654f * 1.4426950408889634f;
  float u = x * (1.0f + 0.044715f * x * x);
  float e = __builtin_amdgcn_exp2f(c * u);
  return x * __builtin_amdgcn_rcpf(1.0f + e);
}
DEV int bswz(int k) { return ((k >> 1) & 1) | (((k >> 3) & 1) << 1); }

#define STAMP(kid, i) do { } while (0)
#define DBG(kid, w, i) do { } while (0)

template <int I, int N, class F>
DEV void static_for(F&& f) {
  if constexpr (I < N) {
    f(std::integral_constant<int, I>{});
    static_for<I + 1, N>(f);
  }
}

DEV void ln_consts(float s, float q, float& mb, float& sd, float& m, float& v) {
  m = s * (1.0f / DM);
  v = fmaxf(q * (1.0f / DM) - m * m, 0.f);
  mb = rbf(m);
  sd = rbf(__builtin_sqrtf(rbf(rbf(v) + 1e-5f)));
}

constexpr int NLD = 4;
template <int MODE>
struct Cfg {
  static constexpr int NT = (MODE == 1 || MODE == 6) ? 3 : 1;
  static constexpr bool LN = (MODE == 1 || MODE == 4 || MODE == 6);
  static constexpr bool PST = (MODE == 4 || MODE == 5 || MODE == 6);
  static constexpr int K = (MODE == 7) ? FF : DM;
  static constexpr int LDA = K;
  static constexpr int LDW = (MODE == 1) ? 3 * DM : (MODE == 6 ? FF : DM);
  static constexpr int NSTAGE = (K + 63) / 64;
  static constexpr int SPB = (NT == 1) ? 2 : 1;
  static constexpr int NB = (NSTAGE + SPB - 1) / SPB;
  static constexpr int RS = 128 * NT;
  static constexpr int BBYTES = 64 * RS;
  static constexpr int SLOT = BBYTES + 8192;
  static constexpr int NGSLOT = 4;
  static constexpr int NSLOT = NGSLOT * SPB;
  static constexpr int DEPTH = NGSLOT - 1;
  static constexpr int X_ST = NSLOT * SLOT;
  static constexpr int X_CS = X_ST + 512;
  static constexpr int LDS = X_CS + 1536;
  static constexpr int LPS = 8 * (NT + 1) / NLD;
};

struct GArgs {
  const u16* A;
  const u16* W;
  const u16* r0;
  const u16* r1;
  const float* pin_;
  float* pout;
  u16* o0;
  u16* o1;
  u16* o2;
};

template <int MODE>
__global__ __launch_bounds__(512 + 64 * NLD, 2) void gemm_kernel(GArgs ga) {
  using C = Cfg<MODE>;
  constexpr int NT = C::NT;
  extern __shared__ __attribute__((aligned(16))) char lds[];
  STAMP(MODE, 0);
  const int tid = threadIdx.x, lane = tid & 63;
  const int wid = __builtin_amdgcn_readfirstlane(tid >> 6);
  const int xcd = blockIdx.x & 7, idx = blockIdx.x >> 3;
  const int rb = xcd * 4 + (idx >> 3), cb = idx & 7;
  const int row0 = rb * 64;
  const unsigned lds0 = (unsigned)(uintptr_t)lds;

  if (wid >= 8) {
    const int L = wid - 8;
    constexpr int IA = 8 / NLD, IB = 8 * NT / NLD;
    unsigned boff[IB];
    int bk[IB];
#pragma unroll
    for (int j = 0; j < IB; ++j) {
      int o = (L * IB + j) * 1024 + lane * 16;
      int k = o / C::RS, rem = o % C::RS;
      int grp = rem >> 7, blk = (rem >> 5) & 3, hf = (rem >> 4) & 1;
      int col;
      if (MODE == 1) col = grp * 512 + cb * 64;
      else col = cb * (64 * NT) + grp * 64;
      col += ((blk ^ bswz(k)) * 16) + hf * 8;
      if (MODE == 6 && col > FF - 8) col = FF - 8;
      bk[j] = k;
      boff[j] = (unsigned)(k * C::LDW + col) * 2u;
    }
    unsigned aoffj[IA];
#pragma unroll
    for (int j = 0; j < IA; ++j) {
      int r = (L * IA + j) * 8 + (lane >> 3), c = (lane & 7) ^ ((r >> 1) & 7);
      aoffj[j] = (unsigned)((row0 + r) * C::LDA + c * 8) * 2u;
    }
    auto issue_sub = [&](int s) {
      const unsigned slot = lds0 + (unsigned)((s % C::NSLOT) * C::SLOT);
#pragma unroll
      for (int j = 0; j < IA; ++j) glds16(ga.A, aoffj[j] + s * 128, slot + C::BBYTES + (L * IA + j) * 1024);
#pragma unroll
      for (int j = 0; j < IB; ++j) {
        unsigned off = boff[j] + (unsigned)(s * 64 * C::LDW) * 2u;
        if (MODE == 7 && s == C::NSTAGE - 1 && s * 64 + bk[j] >= FF) off = boff[j] + (unsigned)((FF - 64) * C::LDW) * 2u;
        glds16(ga.W, off, slot + (unsigned)(L * IB + j) * 1024u);
      }
    };
    auto issue_group = [&](auto g_) {
      constexpr int gg = g_.value;
      static_for<0, C::SPB>([&](auto u_) { if constexpr (gg < C::NB && gg * C::SPB + u_.value < C::NSTAGE) issue_sub(gg * C::SPB + u_.value); });
    };
    issue_group(std::integral_constant<int, 0>{});
    issue_group(std::integral_constant<int, 1>{});
    static_for<0, C::NB>([&](auto b_) {
      constexpr int b = b_.value;
      constexpr int lastg0 = (b == 0) ? 1 : b + C::DEPTH - 1;
      constexpr int lastg = lastg0 < C::NB - 1 ? lastg0 : C::NB - 1;
      constexpr int sub_after = ((lastg + 1) * C::SPB > C::NSTAGE ? C::NSTAGE : (lastg + 1) * C::SPB) - ((b + 1) * C::SPB > C::NSTAGE ? C::NSTAGE : (b + 1) * C::SPB);
      asm volatile("s_waitcnt vmcnt(%0)" ::"i"(sub_after * C::LPS) : "memory");
      if (wid == 8) DBG(MODE, 1, b);
      wait_vm_bar<sub_after * C::LPS>();
      if constexpr (b == 0) {
        static_for<2, C::DEPTH + 1>([&](auto g_) { issue_group(g_); });
      } else {
        issue_group(std::integral_constant<int, b + C::DEPTH>{});
      }
    });
    return;
  }

  const int wm = wid & 1, wn = wid >> 1;
  const int l15 = lane & 15, g = lane >> 4;
  u32x4 ps[2];
  u32x2 res0[2], res1[2];
  if constexpr (C::PST) {
    const float* pp = ga.pin_ + (size_t)(row0 + (tid >> 3)) * 64 + (tid & 7) * 8;
    ps[0] = *(const u32x4*)pp;
    ps[1] = *(const u32x4*)(pp + 4);
  }
  if constexpr (MODE == 3 || MODE == 5 || MODE == 7) {
#pragma unroll
    for (int mt = 0; mt < 2; ++mt) {
      size_t o = (size_t)(row0 + wm * 32 + mt * 16 + l15) * DM + cb * 64 + wn * 16 + 4 * g;
      res0[mt] = *(const u32x2*)(ga.r0 + o);
      if constexpr (MODE == 7) res1[mt] = *(const u32x2*)(ga.r1 + o);
    }
  }

  int xoff[2][2];
#pragma unroll
  for (int mt = 0; mt < 2; ++mt)
#pragma unroll
    for (int kk = 0; kk < 2; ++kk) {
      int r = wm * 32 + mt * 16 + l15;
      xoff[mt][kk] = C::BBYTES + r * 128 + (((4 * kk + g) ^ ((r >> 1) & 7)) * 16);
    }
  int woff[2][2];
  {
    int q = l15 >> 2, p = l15 & 3;
#pragma unroll
    for (int kk = 0; kk < 2; ++kk)
#pragma unroll
      for (int t = 0; t < 2; ++t) {
        int k = 32 * kk + 8 * g + 4 * t + q;
        woff[kk][t] = k * C::RS + ((wn ^ bswz(k)) * 32) + p * 8;
      }
  }

  f32x4 acc[2][NT];
  f32x4 cs[NT];
  const bf16x8 ones8 = {0x3F80, 0x3F80, 0x3F80, 0x3F80, 0x3F80, 0x3F80, 0x3F80, 0x3F80};
#pragma unroll
  for (int nt = 0; nt < NT; ++nt) {
    cs[nt] = f32x4{0.f, 0.f, 0.f, 0.f};
#pragma unroll
    for (int mt = 0; mt < 2; ++mt) acc[mt][nt] = f32x4{0.f, 0.f, 0.f, 0.f};
  }
  f32x2* st_lds = (f32x2*)(lds + C::X_ST);

    {
      if constexpr (C::PST) {
        float s_ = (__uint_as_float(ps[0][0]) + __uint_as_float(ps[0][2])) + (__uint_as_float(ps[1][0]) + __uint_as_float(ps[1][2]));
        float q_ = (__uint_as_float(ps[0][1]) + __uint_as_float(ps[0][3])) + (__uint_as_float(ps[1][1]) + __uint_as_float(ps[1][3]));
        s_ = dpp_add<0xB1>(s_); q_ = dpp_add<0xB1>(q_);
        s_ = dpp_add<0x4E>(s_); q_ = dpp_add<0x4E>(q_);
        s_ = dpp_add<0x141>(s_); q_ = dpp_add<0x141>(q_);
        float mb, sd, m, v;
        ln_consts(s_, q_, mb, sd, m, v);
        float M = mb, R = 1.0f / sd;
        if constexpr (MODE == 4) {
          float m2 = (m - mb) * R, v2 = v * R * R;
          float m2b = rbf(m2);
          float sd2 = rbf(__builtin_sqrtf(rbf(rbf(v2) + 1e-5f)));
          M = mb + m2b * sd;
          R = R / sd2;
        }
        if ((tid & 7) == 0) st_lds[tid >> 3] = f32x2{M, R};
      }
    }
  float sx[2] = {0.f, 0.f}, qx[2] = {0.f, 0.f};
  static_for<0, C::NB>([&](auto b_) {
    constexpr int s = b_.value;
    if (wid == 0) DBG(MODE, 2, s);
    lds_bar();
    if (wid == 0) DBG(MODE, 0, s);
    if constexpr (s == 0) STAMP(MODE, 1);
    constexpr int S0 = s * C::SPB;
    constexpr int NSUB = ((S0 + C::SPB > C::NSTAGE) ? C::NSTAGE : S0 + C::SPB) - S0;
    constexpr int NKS = 2 * NSUB - ((MODE == 7 && S0 + NSUB == C::NSTAGE) ? 1 : 0);
    bf16x8 xf[NKS][2], wf[NKS][NT];
#pragma unroll
    for (int ks = 0; ks < NKS; ++ks) {
      const lds_cptr bs = (lds_cptr)lds + ((S0 + (ks >> 1)) % C::NSLOT) * C::SLOT;
      const int kk = ks & 1;
#pragma unroll
      for (int nt = 0; nt < NT; ++nt) {
        s16x4 w0 = lds_tr(bs + woff[kk][0] + nt * 128);
        s16x4 w1 = lds_tr(bs + woff[kk][1] + nt * 128);
        wf[ks][nt] = __builtin_shufflevector(w0, w1, 0, 1, 2, 3, 4, 5, 6, 7);
        if (nt == 0) {
#pragma unroll
          for (int mt = 0; mt < 2; ++mt) xf[ks][mt] = lds_rd128(bs + xoff[mt][kk]);
        }
      }
    }
#pragma unroll
    for (int ks = 0; ks < NKS; ++ks) {
#pragma unroll
      for (int nt = 0; nt < NT; ++nt) {
#pragma unroll
        for (int mt = 0; mt < 2; ++mt) {
          if (MODE == 1 && nt == 2) acc[mt][nt] = MFMA16(xf[ks][mt], wf[ks][nt], acc[mt][nt]);
          else acc[mt][nt] = MFMA16(wf[ks][nt], xf[ks][mt], acc[mt][nt]);
        }
        if constexpr (C::LN) {
          if (MODE == 1 && nt == 2) cs[nt] = MFMA16(ones8, wf[ks][nt], cs[nt]);
          else cs[nt] = MFMA16(wf[ks][nt], ones8, cs[nt]);
        }
      }
    }
    if constexpr (MODE == 1) {
      auto stat = [&](const bf16x8 (&xx)[2]) {
#pragma unroll
        for (int mt = 0; mt < 2; ++mt) {
          u32x4 xu = __builtin_bit_cast(u32x4, xx[mt]);
#pragma unroll
          for (int e = 0; e < 4; ++e) {
            sx[mt] = dot2_ones(xu[e], sx[mt]);
            qx[mt] = dot2u(xu[e], xu[e], qx[mt]);
          }
        }
      };
      if (wn == ((2 * s) & 3)) stat(xf[0]);
      else if (wn == ((2 * s + 1) & 3)) stat(xf[1]);
    }
    if constexpr (MODE == 1) { pin(sx[0]); pin(sx[1]); pin(qx[0]); pin(qx[1]); }
    __builtin_amdgcn_sched_barrier(0);
  });

  if constexpr (MODE == 1) {
    f32x2* px = (f32x2*)lds;
    float a0 = add_x32(add_x16(sx[0])), b0 = add_x32(add_x16(qx[0])), a1 = add_x32(add_x16(sx[1])), b1 = add_x32(add_x16(qx[1]));
    lds_bar();
    if (g == 0) { px[wn * 64 + wm * 32 + l15] = f32x2{a0, b0}; px[wn * 64 + wm * 32 + 16 + l15] = f32x2{a1, b1}; }
    lds_bar();
    if (tid < 64) {
      f32x2 t = (px[tid] + px[64 + tid]) + (px[128 + tid] + px[192 + tid]);
      float mb, sd, m, v;
      ln_consts(t[0], t[1], mb, sd, m, v);
      st_lds[tid] = f32x2{mb, 1.0f / sd};
    }
    lds_bar();
  }
  STAMP(MODE, 2);
#pragma unroll
  for (int mt = 0; mt < 2; ++mt) {
    const int lrow = wm * 32 + mt * 16 + l15;
    const int tok = row0 + lrow;
#pragma unroll
    for (int nt = 0; nt < NT; ++nt) {
      f32x4 a = acc[mt][nt];
      if constexpr (C::LN) {
        if (MODE == 1 && nt == 2) {
          float c = cs[nt][0];
#pragma unroll
          for (int i = 0; i < 4; ++i) {
            f32x2 st = st_lds[wm * 32 + mt * 16 + 4 * g + i];
            a[i] = (a[i] - st[0] * c) * st[1];
          }
        } else {
          f32x4 c = cs[nt];
          f32x2 st = st_lds[lrow];
#pragma unroll
          for (int i = 0; i < 4; ++i) a[i] = (a[i] - st[0] * c[i]) * st[1];
        }
      }
      if constexpr (MODE == 1) {
        const int head = cb * 4 + wn;
        if (nt < 2) {
          float sc = (nt == 0) ? 0.25f * 1.4426950408889634f : 1.0f;
          u32x2 v = {cvtpk(a[0] * sc, a[1] * sc), cvtpk(a[2] * sc, a[3] * sc)};
          u16* dst = (nt == 0 ? ga.o0 : ga.o1) + ((size_t)(head * 64 + (tok >> 5)) * 2 + (g >> 1)) * 256 + (tok & 31) * 8 + (g & 1) * 4;
          *(u32x2*)dst = v;
        } else {
          const int step = (row0 + wm * 32 + mt * 16) >> 4;
          u32x2 v = {cvtpk(a[0], a[1]), cvtpk(a[2], a[3])};
          u16* dst = ga.o2 + ((size_t)(head * 128 + step) * 2 + (g & 1)) * 128 + l15 * 8 + (g >> 1) * 4;
          *(u32x2*)dst = v;
        }
      } else if constexpr (MODE == 3 || MODE == 5) {
        const int col = cb * 64 + wn * 16 + 4 * g;
        u32x2 r = res0[mt];
        if constexpr (MODE == 5) {
          f32x2 st = st_lds[lrow];
          unsigned d0 = cvtpk(lo2f(r[0]) - st[0], hi2f(r[0]) - st[0]), d1 = cvtpk(lo2f(r[1]) - st[0], hi2f(r[1]) - st[0]);
          r[0] = cvtpk(lo2f(d0) * st[1], hi2f(d0) * st[1]);
          r[1] = cvtpk(lo2f(d1) * st[1], hi2f(d1) * st[1]);
        }
        unsigned a0 = cvtpk(a[0], a[1]), a1 = cvtpk(a[2], a[3]);
        u32x2 v = {cvtpk(lo2f(r[0]) + lo2f(a0), hi2f(r[0]) + hi2f(a0)), cvtpk(lo2f(r[1]) + lo2f(a1), hi2f(r[1]) + hi2f(a1))};
        *(u32x2*)(ga.o0 + (size_t)tok * DM + col) = v;
        float f0 = lo2f(v[0]), f1 = hi2f(v[0]), f2 = lo2f(v[1]), f3 = hi2f(v[1]);
        float s_ = (f0 + f1) + (f2 + f3), q_ = fmaf(f0, f0, f1 * f1) + fmaf(f2, f2, f3 * f3);
        s_ = add_x32(add_x16(s_)); q_ = add_x32(add_x16(q_));
        if (g == 0) *(f32x2*)(ga.pout + (size_t)tok * 64 + (cb * 4 + wn) * 2) = f32x2{s_, q_};
      } else if constexpr (MODE == 4) {
        const int col = cb * 64 + wn * 16 + 4 * g;
        unsigned a0 = cvtpk(a[0], a[1]), a1 = cvtpk(a[2], a[3]);
        u32x2 v = {cvtpk(gelu_tanh(lo2f(a0)), gelu_tanh(hi2f(a0))), cvtpk(gelu_tanh(lo2f(a1)), gelu_tanh(hi2f(a1)))};
        const int grp = cb >> 1;
        const int sh = (grp == 0) ? 1 : (grp == 1) ? -1 : (grp == 2) ? 2 : -2;
        const int dt = tok + sh;
        if (dt >= 0 && dt < SEQ) *(u32x2*)(ga.o0 + (size_t)dt * DM + col) = v;
        const int src = tok - sh;
        if (src < 0 || src >= SEQ) *(u32x2*)(ga.o0 + (size_t)tok * DM + col) = u32x2{0u, 0u};
      } else if constexpr (MODE == 6) {
        const int col = cb * 192 + nt * 64 + wn * 16 + 4 * g;
        unsigned a0 = cvtpk(a[0], a[1]), a1 = cvtpk(a[2], a[3]);
        u32x2 v = {cvtpk(gelu_tanh(lo2f(a0)), gelu_tanh(hi2f(a0))), cvtpk(gelu_tanh(lo2f(a1)), gelu_tanh(hi2f(a1)))};
        if (col < FF) *(u32x2*)(ga.o0 + (size_t)tok * FF + col) = v;
      } else {
        const int col = cb * 64 + wn * 16 + 4 * g;
        u32x2 x1 = res0[mt], hh = res1[mt];
        unsigned a0 = cvtpk(a[0], a[1]), a1 = cvtpk(a[2], a[3]);
        unsigned b0 = cvtpk(lo2f(x1[0]) + lo2f(a0), hi2f(x1[0]) + hi2f(a0));
        unsigned b1 = cvtpk(lo2f(x1[1]) + lo2f(a1), hi2f(x1[1]) + hi2f(a1));
        u32x2 v = {cvtpk(lo2f(hh[0]) + lo2f(b0), hi2f(hh[0]) + hi2f(b0)), cvtpk(lo2f(hh[1]) + lo2f(b1), hi2f(hh[1]) + hi2f(b1))};
        *(u32x2*)(ga.o0 + (size_t)tok * DM + col) = v;
      }
    }
  }
  STAMP(MODE, 3);
}

constexpr int AT_K = 0, AT_V = 65536, AT_ONES = 131072, AT_Q = AT_ONES + 8192, AT_LDS = AT_Q + 8192;

__global__ __launch_bounds__(512, 2) void attn_kernel(const u16* Qf, const u16* Kf, const u16* Vf, u16* O, const u16* wa,
                                                       const u16* wb, const u16* wc, const u16* wd, const u16* we) {
  extern __shared__ __attribute__((aligned(16))) char lds[];
  STAMP(2, 0);
  const int tid = threadIdx.x, lane = tid & 63;
  const int wid = __builtin_amdgcn_readfirstlane(tid >> 6);
  const int xcd = blockIdx.x & 7, idx = blockIdx.x >> 3;
  const int head = xcd * 4 + (idx >> 3), qb = idx & 7;
  const unsigned lds0 = (unsigned)(uintptr_t)lds;
  const int r = lane & 31, h = lane >> 5;
  const char* kg = (const char*)Kf + (size_t)head * 65536;
  const char* vg = (const char*)Vf + (size_t)head * 65536;
  const char* qg = (const char*)Qf + (size_t)head * 65536 + (qb * 8 + wid) * 1024;
  const unsigned lo = lane * 16;

  glds16(qg, lo, lds0 + AT_Q + wid * 1024);
#pragma unroll
  for (int c = 0; c < 8; ++c) {
    glds16(kg, lo + c * 8192 + wid * 1024, lds0 + AT_K + c * 8192 + wid * 1024);
    glds16(vg, lo + c * 8192 + wid * 1024, lds0 + AT_V + c * 8192 + wid * 1024);
  }
  *(u32x4*)(lds + AT_ONES + tid * 16) = u32x4{0x3F803F80u, 0x3F803F80u, 0x3F803F80u, 0x3F803F80u};

  lds_cptr kp = (lds_cptr)lds + AT_K + lane * 16;
  lds_cptr vp = (lds_cptr)lds + ((r < 16) ? (AT_V + h * 256 + r * 16) : (AT_ONES + (r - 16) * 16));
  const int vstep = (r < 16) ? 4096 : 0;

  f32x16 o, negm, sA, sB;
#pragma unroll
  for (int i = 0; i < 16; ++i) { o[i] = 0.f; negm[i] = 0.f; }

  wait_vm_bar<10>();
  STAMP(2, 1);
  unsigned tw[5];
  {
    unsigned t = (unsigned)(idx * 512 + tid) * 128u;
    unsigned ta = t < 512u * 1024u ? t : 0u, tb = t < (unsigned)(DM * FF * 2) ? t : 0u;
    asm volatile("global_load_dword %0, %5, %7\n\tglobal_load_dword %1, %5, %8\n\tglobal_load_dword %2, %5, %9\n\t"
                 "global_load_dword %3, %6, %10\n\tglobal_load_dword %4, %6, %11"
                 : "=&v"(tw[0]), "=&v"(tw[1]), "=&v"(tw[2]), "=&v"(tw[3]), "=&v"(tw[4])
                 : "v"(ta), "v"(tb), "s"(wa), "s"(wb), "s"(wc), "s"(wd), "s"(we)
                 : "memory");
  }
  const bf16x8 qf = lds_rd128((lds_cptr)lds + AT_Q + wid * 1024 + lane * 16);
  bf16x8 kfA, kfB, vA0, vA1, vB0, vB1;
  u32x4 pA0, pA1, pB0, pB1;
  {
    sA = MFMA32(lds_rd128(kp), qf, negm);
    kfA = lds_rd128(kp + 1024);
    float mx = sA[0];
#pragma unroll
    for (int i = 1; i < 16; ++i) mx = fmaxf(mx, sA[i]);
    const float d = max_x32(mx) + 7.0f;
#pragma unroll
    for (int i = 0; i < 16; ++i) { negm[i] = -d; sA[i] -= d; }
    vB0 = lds_rd128(vp); vB1 = vB0;
    pB0 = u32x4{0u, 0u, 0u, 0u}; pB1 = pB0;
  }

#define SBAR() __builtin_amdgcn_sched_barrier(0)
#define PK(x, i) __builtin_amdgcn_perm(__float_as_uint(x[(i) + 1]), __float_as_uint(x[i]), 0x07060302u)
#define EXPS(lo_, hi_) _Pragma("unroll") for (int i = lo_; i < hi_; ++i) sc[i] = __builtin_amdgcn_exp2f(sc[i])
  auto step = [&](f32x16& sc, f32x16& sn, const bf16x8& kf, bf16x8& kfn, const bf16x8& vp0, const bf16x8& vp1,
                  const u32x4& pp0, const u32x4& pp1, bf16x8& vc0, bf16x8& vc1, u32x4& pc0, u32x4& pc1, lds_cptr kc,
                  lds_cptr vc) {
    sn = MFMA32(kf, qf, negm);
    kfn = lds_rd128(kc + 2048);
    vc0 = lds_rd128(vc); vc1 = lds_rd128(vc + 512);
    SBAR();
    EXPS(0, 5);
    SBAR();
    o = MFMA32(vp0, __builtin_bit_cast(bf16x8, pp0), o);
    SBAR();
    EXPS(5, 11);
    SBAR();
    o = MFMA32(vp1, __builtin_bit_cast(bf16x8, pp1), o);
    SBAR();
    EXPS(11, 16);
    SBAR();
#pragma unroll
    for (int i = 0; i < 4; ++i) { pc0[i] = PK(sc, 2 * i); pc1[i] = PK(sc, 8 + 2 * i); }
    unsigned chk = (pc0[0] | pc0[1] | pc0[2]) | (pc0[3] | pc1[0] | pc1[1]) | (pc1[2] | pc1[3]);
    if (__builtin_expect(__any((chk & 0x40004000u) != 0), 0)) {
      f32x16 s2 = MFMA32(lds_rd128(kc), qf, negm);
      float mx = s2[0];
#pragma unroll
      for (int i = 1; i < 16; ++i) mx = fmaxf(mx, s2[i]);
      const float d = fmaxf(max_x32(mx) + 7.0f, 0.f);
      const float alpha = __builtin_amdgcn_exp2f(-d);
#pragma unroll
      for (int i = 0; i < 16; ++i) { o[i] *= alpha; negm[i] -= d; sn[i] -= d; s2[i] = __builtin_amdgcn_exp2f(s2[i] - d); }
#pragma unroll
      for (int i = 0; i < 4; ++i) { pc0[i] = PK(s2, 2 * i); pc1[i] = PK(s2, 8 + 2 * i); }
    }
    SBAR();
  };
  auto group = [&]() {
    step(sA, sB, kfA, kfB, vB0, vB1, pB0, pB1, vA0, vA1, pA0, pA1, kp, vp);
    step(sB, sA, kfB, kfA, vA0, vA1, pA0, pA1, vB0, vB1, pB0, pB1, kp + 1024, vp + 1024);
    step(sA, sB, kfA, kfB, vB0, vB1, pB0, pB1, vA0, vA1, pA0, pA1, kp + 2048, vp + 2048);
    step(sB, sA, kfB, kfA, vA0, vA1, pA0, pA1, vB0, vB1, pB0, pB1, kp + 3072, vp + 3072);
    kp += 4096;
    vp += vstep;
  };
#pragma unroll 1
  for (int it = 0; it < 4; ++it) group();
  asm volatile("s_waitcnt vmcnt(0) lgkmcnt(0)\n\ts_barrier" : "+v"(tw[0]), "+v"(tw[1]), "+v"(tw[2]), "+v"(tw[3]), "+v"(tw[4])::"memory");
#pragma unroll 1
  for (int it = 4; it < 16; ++it) group();
  o = MFMA32(vB0, __builtin_bit_cast(bf16x8, pB0), o);
  o = MFMA32(vB1, __builtin_bit_cast(bf16x8, pB1), o);

  STAMP(2, 2);
  const float inv = 1.0f / o[8];
  const int q = qb * 256 + wid * 32 + r;
  u16* dst = O + (size_t)q * DM + head * 16 + 4 * h;
  *(u32x2*)dst = u32x2{cvtpk(o[0] * inv, o[1] * inv), cvtpk(o[2] * inv, o[3] * inv)};
  *(u32x2*)(dst + 8) = u32x2{cvtpk(o[4] * inv, o[5] * inv), cvtpk(o[6] * inv, o[7] * inv)};
  STAMP(2, 3);
}

template <int MODE>
static void launch_gemm(const GArgs& ga, hipStream_t stream) {
  (void)hipFuncSetAttribute((const void*)gemm_kernel<MODE>, hipFuncAttributeMaxDynamicSharedMemorySize, Cfg<MODE>::LDS);
  gemm_kernel<MODE><<<256, 512 + 64 * NLD, Cfg<MODE>::LDS, stream>>>(ga);
}

extern "C" void kernel_launch(void* const* d_in, const int* in_sizes, int n_in, void* d_out, int out_size, void* d_ws,
                              size_t ws_size, hipStream_t stream) {
  const u16* x = (const u16*)d_in[0];
  const u16* wqkv = (const u16*)d_in[1];
  const u16* wo = (const u16*)d_in[2];
  const u16* w1 = (const u16*)d_in[4];
  const u16* w2 = (const u16*)d_in[5];
  const u16* w3 = (const u16*)d_in[6];
  const u16* w4 = (const u16*)d_in[7];
  char* ws = (char*)d_ws;
  u16 *Q = (u16*)(ws + WS_Q), *Kf = (u16*)(ws + WS_K), *Vf = (u16*)(ws + WS_V), *O = (u16*)(ws + WS_O),
      *H = (u16*)(ws + WS_H), *T = (u16*)(ws + WS_T), *X1 = (u16*)(ws + WS_X1), *U = (u16*)(ws + WS_U);
  float *PH = (float*)(ws + WS_PH), *PX = (float*)(ws + WS_PX);
  u16* out = (u16*)d_out;

  launch_gemm<1>(GArgs{x, wqkv, nullptr, nullptr, nullptr, nullptr, Q, Kf, Vf}, stream);
  (void)hipFuncSetAttribute((const void*)attn_kernel, hipFuncAttributeMaxDynamicSharedMemorySize, AT_LDS);
  attn_kernel<<<256, 512, AT_LDS, stream>>>(Q, Kf, Vf, O, wo, w1, w2, w3, w4);
  launch_gemm<3>(GArgs{O, wo, x, nullptr, nullptr, PH, H, nullptr, nullptr}, stream);
  launch_gemm<4>(GArgs{H, w1, nullptr, nullptr, PH, nullptr, T, nullptr, nullptr}, stream);
  launch_gemm<5>(GArgs{T, w2, H, nullptr, PH, PX, X1, nullptr, nullptr}, stream);
  launch_gemm<6>(GArgs{X1, w3, nullptr, nullptr, PX, nullptr, U, nullptr, nullptr}, stream);
  launch_gemm<7>(GArgs{U, w4, X1, H, nullptr, nullptr, out, nullptr, nullptr}, stream);
}
```

```cpp
#include <hip/hip_runtime.h>
#include <hip/hip_bf16.h>
#include <stdint.h>

typedef unsigned short u16;
typedef __attribute__((ext_vector_type(8))) short bf16x8;
typedef __attribute__((ext_vector_type(4))) short s16x4;
typedef __attribute__((ext_vector_type(2))) float f32x2;
typedef __attribute__((ext_vector_type(4))) float f32x4;
typedef __attribute__((ext_vector_type(16))) float f32x16;
typedef __attribute__((ext_vector_type(2))) __bf16 bf16x2_t;
typedef __attribute__((ext_vector_type(4))) unsigned u32x4;
typedef __attribute__((ext_vector_type(2))) unsigned u32x2;
typedef short v4i16_t __attribute__((ext_vector_type(4)));
typedef __attribute__((address_space(3))) const char* lds_cptr;

#define DEV __device__ __forceinline__
#define MFMA16(a, b, c) __builtin_amdgcn_mfma_f32_16x16x32_bf16((a), (b), (c), 0, 0, 0)
#define MFMA32(a, b, c) __builtin_amdgcn_mfma_f32_32x32x16_bf16((a), (b), (c), 0, 0, 0)

constexpr int SEQ = 2048, DM = 512, NH = 32, FF = 1376;

constexpr size_t WS_Q = 0, WS_K = 2u << 20, WS_V = 4u << 20, WS_O = 6u << 20, WS_H = 8u << 20, WS_PH = 10u << 20,
                 WS_PX = 11u << 20, WS_T = 12u << 20, WS_X1 = 14u << 20, WS_U = 16u << 20;

DEV unsigned cvtpk(float lo, float hi) {
  f32x2 v = {lo, hi};
  bf16x2_t b = __builtin_convertvector(v, bf16x2_t);
  return __builtin_bit_cast(unsigned, b);
}
DEV float lo2f(unsigned p) { return __uint_as_float(p << 16); }
DEV float hi2f(unsigned p) { return __uint_as_float(p & 0xffff0000u); }
DEV float rbf(float x) { return lo2f(cvtpk(x, x)); }

DEV void glds16(const void* gbase, unsigned voff, unsigned lds_base) {
  unsigned sv;
  asm volatile("s_mov_b32 %0, m0\n\ts_mov_b32 m0, %3\n\ts_nop 0\n\tglobal_load_lds_dwordx4 %1, %2\n\ts_mov_b32 m0, %0"
               : "=&s"(sv)
               : "v"(voff), "s"(gbase), "s"(lds_base)
               : "memory");
}
DEV u32x4 aload4(const void* gbase, unsigned voff) {
  u32x4 r;
  asm volatile("global_load_dwordx4 %0, %1, %2" : "=v"(r) : "v"(voff), "s"(gbase) : "memory");
  return r;
}
DEV u32x2 aload2(const void* gbase, unsigned voff) {
  u32x2 r;
  asm volatile("global_load_dwordx2 %0, %1, %2" : "=v"(r) : "v"(voff), "s"(gbase) : "memory");
  return r;
}
template <class T>
DEV void pin(T& x) { asm volatile("" : "+v"(x)); }
template <int N>
DEV void wait_vm_bar() {
  asm volatile("s_waitcnt vmcnt(%0) lgkmcnt(0)\n\ts_barrier" ::"i"(N) : "memory");
}
DEV void lds_bar() { asm volatile("s_waitcnt lgkmcnt(0)\n\ts_barrier" ::: "memory"); }
DEV bf16x8 lds_rd128(lds_cptr p) { return *(const __attribute__((address_space(3))) bf16x8*)p; }
DEV s16x4 lds_tr(lds_cptr p) {
  return __builtin_bit_cast(s16x4, __builtin_amdgcn_ds_read_tr16_b64_v4i16((__attribute__((address_space(3))) v4i16_t*)p));
}
template <int CTRL>
DEV float dpp_add(float v) {
  int x = __builtin_amdgcn_update_dpp(0, __float_as_int(v), CTRL, 0xf, 0xf, true);
  return v + __int_as_float(x);
}
DEV float add_x16(float v) {
  auto p = __builtin_amdgcn_permlane16_swap(__float_as_uint(v), __float_as_uint(v), false, false);
  return __uint_as_float(p[0]) + __uint_as_float(p[1]);
}
DEV float add_x32(float v) {
  auto p = __builtin_amdgcn_permlane32_swap(__float_as_uint(v), __float_as_uint(v), false, false);
  return __uint_as_float(p[0]) + __uint_as_float(p[1]);
}
DEV float max_x32(float v) {
  auto p = __builtin_amdgcn_permlane32_swap(__float_as_uint(v), __float_as_uint(v), false, false);
  return fmaxf(__uint_as_float(p[0]), __uint_as_float(p[1]));
}
DEV float wave_sum(float v) {
  v = dpp_add<0x121>(v);
  v = dpp_add<0x122>(v);
  v = dpp_add<0x124>(v);
  v = dpp_add<0x128>(v);
  return add_x32(add_x16(v));
}
DEV float dot2_ones(unsigned p, float acc) {
  return __builtin_amdgcn_fdot2_f32_bf16(__builtin_bit_cast(bf16x2_t, p), __builtin_bit_cast(bf16x2_t, 0x3F803F80u), acc, false);
}
DEV float dot2u(unsigned a, unsigned b, float acc) {
  return __builtin_amdgcn_fdot2_f32_bf16(__builtin_bit_cast(bf16x2_t, a), __builtin_bit_cast(bf16x2_t, b), acc, false);
}
DEV float gelu_tanh(float x) {
  const float c = -2.0f * 0.7978845608028654f * 1.4426950408889634f;
  float u = x * (1.0f + 0.044715f * x * x);
  float e = __builtin_amdgcn_exp2f(c * u);
  return x * __builtin_amdgcn_rcpf(1.0f + e);
}
DEV int bswz(int k) { return ((k >> 1) & 1) | (((k >> 3) & 1) << 1); }

#define STAMP(kid, i) do { } while (0)
#define DBG(kid, w, i) do { } while (0)

template <int I, int N, class F>
DEV void static_for(F&& f) {
  if constexpr (I < N) {
    f(std::integral_constant<int, I>{});
    static_for<I + 1, N>(f);
  }
}

DEV void ln_consts(float s, float q, float& mb, float& sd, float& m, float& v) {
  m = s * (1.0f / DM);
  v = fmaxf(q * (1.0f / DM) - m * m, 0.f);
  mb = rbf(m);
  sd = rbf(__builtin_sqrtf(rbf(rbf(v) + 1e-5f)));
}

constexpr int NLD = 4;
template <int MODE>
struct Cfg {
  static constexpr int NT = (MODE == 1 || MODE == 6) ? 3 : 1;
  static constexpr bool LN = (MODE == 1 || MODE == 4 || MODE == 6);
  static constexpr bool PST = (MODE == 4 || MODE == 5 || MODE == 6);
  static constexpr int K = (MODE == 7) ? FF : DM;
  static constexpr int LDA = K;
  static constexpr int LDW = (MODE == 1) ? 3 * DM : (MODE == 6 ? FF : DM);
  static constexpr int NSTAGE = (K + 63) / 64;
  static constexpr int SPB = (NT == 1) ? 2 : 1;
  static constexpr int NB = (NSTAGE + SPB - 1) / SPB;
  static constexpr int RS = 128 * NT;
  static constexpr int BBYTES = 64 * RS;
  static constexpr int SLOT = BBYTES + 8192;
  static constexpr int NGSLOT = 4;
  static constexpr int NSLOT = NGSLOT * SPB;
  static constexpr int DEPTH = NGSLOT - 1;
  static constexpr int X_ST = NSLOT * SLOT;
  static constexpr int X_CS = X_ST + 512;
  static constexpr int X_PX = X_CS + 1536;
  static constexpr int X_OUT = X_PX + 2048;
  static constexpr int RSO = 128 * NT + 16;
  static constexpr int LDS = X_OUT + (MODE == 1 ? 0 : 64 * RSO);
  static constexpr int LPS = 8 * (NT + 1) / NLD;
};

struct GArgs {
  const u16* A;
  const u16* W;
  const u16* r0;
  const u16* r1;
  const float* pin_;
  float* pout;
  u16* o0;
  u16* o1;
  u16* o2;
};

template <int MODE>
__global__ __launch_bounds__(512 + 64 * NLD, 2) void gemm_kernel(GArgs ga) {
  using C = Cfg<MODE>;
  constexpr int NT = C::NT;
  extern __shared__ __attribute__((aligned(16))) char lds[];
  STAMP(MODE, 0);
  const int tid = threadIdx.x, lane = tid & 63;
  const int wid = __builtin_amdgcn_readfirstlane(tid >> 6);
  const int xcd = blockIdx.x & 7, idx = blockIdx.x >> 3;
  const int rb = xcd * 4 + (idx >> 3), cb = idx & 7;
  const int row0 = rb * 64;
  const unsigned lds0 = (unsigned)(uintptr_t)lds;

  if (wid >= 8) {
    const int L = wid - 8;
    constexpr int IA = 8 / NLD, IB = 8 * NT / NLD;
    unsigned boff[IB];
    int bk[IB];
#pragma unroll
    for (int j = 0; j < IB; ++j) {
      int o = (L * IB + j) * 1024 + lane * 16;
      int k = o / C::RS, rem = o % C::RS;
      int grp = rem >> 7, blk = (rem >> 5) & 3, hf = (rem >> 4) & 1;
      int col;
      if (MODE == 1) col = grp * 512 + cb * 64;
      else col = cb * (64 * NT) + grp * 64;
      col += ((blk ^ bswz(k)) * 16) + hf * 8;
      if (MODE == 6 && col > FF - 8) col = FF - 8;
      bk[j] = k;
      boff[j] = (unsigned)(k * C::LDW + col) * 2u;
    }
    unsigned aoffj[IA];
#pragma unroll
    for (int j = 0; j < IA; ++j) {
      int r = (L * IA + j) * 8 + (lane >> 3), c = (lane & 7) ^ ((r >> 1) & 7);
      aoffj[j] = (unsigned)((row0 + r) * C::LDA + c * 8) * 2u;
    }
    auto issue_sub = [&](int s) {
      const unsigned slot = lds0 + (unsigned)((s % C::NSLOT) * C::SLOT);
#pragma unroll
      for (int j = 0; j < IA; ++j) glds16(ga.A, aoffj[j] + s * 128, slot + C::BBYTES + (L * IA + j) * 1024);
#pragma unroll
      for (int j = 0; j < IB; ++j) {
        unsigned off = boff[j] + (unsigned)(s * 64 * C::LDW) * 2u;
        if (MODE == 7 && s == C::NSTAGE - 1 && s * 64 + bk[j] >= FF) off = boff[j] + (unsigned)((FF - 64) * C::LDW) * 2u;
        glds16(ga.W, off, slot + (unsigned)(L * IB + j) * 1024u);
      }
    };
    auto issue_group = [&](auto g_) {
      constexpr int gg = g_.value;
      static_for<0, C::SPB>([&](auto u_) { if constexpr (gg < C::NB && gg * C::SPB + u_.value < C::NSTAGE) issue_sub(gg * C::SPB + u_.value); });
    };
    issue_group(std::integral_constant<int, 0>{});
    issue_group(std::integral_constant<int, 1>{});
    static_for<0, C::NB>([&](auto b_) {
      constexpr int b = b_.value;
      constexpr int lastg0 = (b == 0) ? 1 : b + C::DEPTH - 1;
      constexpr int lastg = lastg0 < C::NB - 1 ? lastg0 : C::NB - 1;
      constexpr int sub_after = ((lastg + 1) * C::SPB > C::NSTAGE ? C::NSTAGE : (lastg + 1) * C::SPB) - ((b + 1) * C::SPB > C::NSTAGE ? C::NSTAGE : (b + 1) * C::SPB);
      asm volatile("s_waitcnt vmcnt(%0)" ::"i"(sub_after * C::LPS) : "memory");
      if (wid == 8) DBG(MODE, 1, b);
      wait_vm_bar<sub_after * C::LPS>();
      if constexpr (b == 0) {
        static_for<2, C::DEPTH + 1>([&](auto g_) { issue_group(g_); });
      } else {
        issue_group(std::integral_constant<int, b + C::DEPTH>{});
      }
    });
    return;
  }

  const int wm = wid & 1, wn = wid >> 1;
  const int l15 = lane & 15, g = lane >> 4;
  u32x4 ps[2];
  u32x2 res0[2], res1[2];
  if constexpr (C::PST) {
    const float* pp = ga.pin_ + (size_t)(row0 + (tid >> 3)) * 64 + (tid & 7) * 8;
    ps[0] = *(const u32x4*)pp;
    ps[1] = *(const u32x4*)(pp + 4);
  }
  if constexpr (MODE == 3 || MODE == 5 || MODE == 7) {
#pragma unroll
    for (int mt = 0; mt < 2; ++mt) {
      size_t o = (size_t)(row0 + wm * 32 + mt * 16 + l15) * DM + cb * 64 + wn * 16 + 4 * g;
      res0[mt] = *(const u32x2*)(ga.r0 + o);
      if constexpr (MODE == 7) res1[mt] = *(const u32x2*)(ga.r1 + o);
    }
  }

  int xoff[2][2];
#pragma unroll
  for (int mt = 0; mt < 2; ++mt)
#pragma unroll
    for (int kk = 0; kk < 2; ++kk) {
      int r = wm * 32 + mt * 16 + l15;
      xoff[mt][kk] = C::BBYTES + r * 128 + (((4 * kk + g) ^ ((r >> 1) & 7)) * 16);
    }
  int woff[2][2];
  {
    int q = l15 >> 2, p = l15 & 3;
#pragma unroll
    for (int kk = 0; kk < 2; ++kk)
#pragma unroll
      for (int t = 0; t < 2; ++t) {
        int k = 32 * kk + 8 * g + 4 * t + q;
        woff[kk][t] = k * C::RS + ((wn ^ bswz(k)) * 32) + p * 8;
      }
  }

  f32x4 acc[2][NT];
  f32x4 cs[NT];
  const bf16x8 ones8 = {0x3F80, 0x3F80, 0x3F80, 0x3F80, 0x3F80, 0x3F80, 0x3F80, 0x3F80};
#pragma unroll
  for (int nt = 0; nt < NT; ++nt) {
    cs[nt] = f32x4{0.f, 0.f, 0.f, 0.f};
#pragma unroll
    for (int mt = 0; mt < 2; ++mt) acc[mt][nt] = f32x4{0.f, 0.f, 0.f, 0.f};
  }
  f32x2* st_lds = (f32x2*)(lds + C::X_ST);

    {
      if constexpr (C::PST) {
        float s_ = (__uint_as_float(ps[0][0]) + __uint_as_float(ps[0][2])) + (__uint_as_float(ps[1][0]) + __uint_as_float(ps[1][2]));
        float q_ = (__uint_as_float(ps[0][1]) + __uint_as_float(ps[0][3])) + (__uint_as_float(ps[1][1]) + __uint_as_float(ps[1][3]));
        s_ = dpp_add<0xB1>(s_); q_ = dpp_add<0xB1>(q_);
        s_ = dpp_add<0x4E>(s_); q_ = dpp_add<0x4E>(q_);
        s_ = dpp_add<0x141>(s_); q_ = dpp_add<0x141>(q_);
        float mb, sd, m, v;
        ln_consts(s_, q_, mb, sd, m, v);
        float M = mb, R = 1.0f / sd;
        if constexpr (MODE == 4) {
          float m2 = (m - mb) * R, v2 = v * R * R;
          float m2b = rbf(m2);
          float sd2 = rbf(__builtin_sqrtf(rbf(rbf(v2) + 1e-5f)));
          M = mb + m2b * sd;
          R = R / sd2;
        }
        if ((tid & 7) == 0) st_lds[tid >> 3] = f32x2{M, R};
      }
    }
  float sx[2] = {0.f, 0.f}, qx[2] = {0.f, 0.f};
  static_for<0, C::NB>([&](auto b_) {
    constexpr int s = b_.value;
    if (wid == 0) DBG(MODE, 2, s);
    lds_bar();
    if (wid == 0) DBG(MODE, 0, s);
    if constexpr (s == 0) STAMP(MODE, 1);
    constexpr int S0 = s * C::SPB;
    constexpr int NSUB = ((S0 + C::SPB > C::NSTAGE) ? C::NSTAGE : S0 + C::SPB) - S0;
    constexpr int NKS = 2 * NSUB - ((MODE == 7 && S0 + NSUB == C::NSTAGE) ? 1 : 0);
    bf16x8 xf[NKS][2], wf[NKS][NT];
#pragma unroll
    for (int ks = 0; ks < NKS; ++ks) {
      const lds_cptr bs = (lds_cptr)lds + ((S0 + (ks >> 1)) % C::NSLOT) * C::SLOT;
      const int kk = ks & 1;
#pragma unroll
      for (int nt = 0; nt < NT; ++nt) {
        s16x4 w0 = lds_tr(bs + woff[kk][0] + nt * 128);
        s16x4 w1 = lds_tr(bs + woff[kk][1] + nt * 128);
        wf[ks][nt] = __builtin_shufflevector(w0, w1, 0, 1, 2, 3, 4, 5, 6, 7);
        if (nt == 0) {
#pragma unroll
          for (int mt = 0; mt < 2; ++mt) xf[ks][mt] = lds_rd128(bs + xoff[mt][kk]);
        }
      }
    }
#pragma unroll
    for (int ks = 0; ks < NKS; ++ks) {
#pragma unroll
      for (int nt = 0; nt < NT; ++nt) {
#pragma unroll
        for (int mt = 0; mt < 2; ++mt) {
          if (MODE == 1 && nt == 2) acc[mt][nt] = MFMA16(xf[ks][mt], wf[ks][nt], acc[mt][nt]);
          else acc[mt][nt] = MFMA16(wf[ks][nt], xf[ks][mt], acc[mt][nt]);
        }
      }
    }
    if constexpr (C::LN) {
      auto colsum = [&](auto par_) {
#pragma unroll
        for (int ks = par_.value; ks < NKS; ks += 2)
#pragma unroll
          for (int nt = 0; nt < NT; ++nt) {
            if (MODE == 1 && nt == 2) cs[nt] = MFMA16(ones8, wf[ks][nt], cs[nt]);
            else cs[nt] = MFMA16(wf[ks][nt], ones8, cs[nt]);
          }
      };
      if (wm == 0) colsum(std::integral_constant<int, 0>{});
      else colsum(std::integral_constant<int, 1>{});
    }
    if constexpr (MODE == 1) {
      auto stat = [&](const bf16x8 (&xx)[2]) {
#pragma unroll
        for (int mt = 0; mt < 2; ++mt) {
          u32x4 xu = __builtin_bit_cast(u32x4, xx[mt]);
#pragma unroll
          for (int e = 0; e < 4; ++e) {
            sx[mt] = dot2_ones(xu[e], sx[mt]);
            qx[mt] = dot2u(xu[e], xu[e], qx[mt]);
          }
        }
      };
      if (wn == ((2 * s) & 3)) stat(xf[0]);
      else if (wn == ((2 * s + 1) & 3)) stat(xf[1]);
    }
    if constexpr (MODE == 1) { pin(sx[0]); pin(sx[1]); pin(qx[0]); pin(qx[1]); }
    __builtin_amdgcn_sched_barrier(0);
  });

  float* cs_lds = (float*)(lds + C::X_CS);
  if constexpr (C::LN) {
#pragma unroll
    for (int nt = 0; nt < NT; ++nt) {
      float* d = cs_lds + ((wm * 4 + wn) * NT + nt) * 16;
      if (MODE == 1 && nt == 2) { if (g == 0) d[l15] = cs[nt][0]; }
      else if (l15 == 0) *(f32x4*)(d + 4 * g) = cs[nt];
    }
    if constexpr (MODE == 1) {
      f32x2* px = (f32x2*)(lds + C::X_PX);
      float a0 = add_x32(add_x16(sx[0])), b0 = add_x32(add_x16(qx[0])), a1 = add_x32(add_x16(sx[1])), b1 = add_x32(add_x16(qx[1]));
      if (g == 0) { px[wn * 64 + wm * 32 + l15] = f32x2{a0, b0}; px[wn * 64 + wm * 32 + 16 + l15] = f32x2{a1, b1}; }
      lds_bar();
      if (tid < 64) {
        f32x2 t = (px[tid] + px[64 + tid]) + (px[128 + tid] + px[192 + tid]);
        float mb, sd, m, v;
        ln_consts(t[0], t[1], mb, sd, m, v);
        st_lds[tid] = f32x2{mb, 1.0f / sd};
      }
    }
    lds_bar();
#pragma unroll
    for (int nt = 0; nt < NT; ++nt) {
      const float* d = cs_lds + (((1 - wm) * 4 + wn) * NT + nt) * 16;
      if (MODE == 1 && nt == 2) cs[nt][0] += d[l15];
      else cs[nt] += *(const f32x4*)(d + 4 * g);
    }
  }
  STAMP(MODE, 2);
  char* ost = lds + C::X_OUT;
#pragma unroll
  for (int mt = 0; mt < 2; ++mt) {
    const int lrow = wm * 32 + mt * 16 + l15;
    const int tok = row0 + lrow;
#pragma unroll
    for (int nt = 0; nt < NT; ++nt) {
      f32x4 a = acc[mt][nt];
      if constexpr (C::LN) {
        if (MODE == 1 && nt == 2) {
          float c = cs[nt][0];
#pragma unroll
          for (int i = 0; i < 4; ++i) {
            f32x2 st = st_lds[wm * 32 + mt * 16 + 4 * g + i];
            a[i] = (a[i] - st[0] * c) * st[1];
          }
        } else {
          f32x4 c = cs[nt];
          f32x2 st = st_lds[lrow];
#pragma unroll
          for (int i = 0; i < 4; ++i) a[i] = (a[i] - st[0] * c[i]) * st[1];
        }
      }
      u32x2 v;
      if constexpr (MODE == 1) {
        const int head = cb * 4 + wn;
        if (nt < 2) {
          float sc = (nt == 0) ? 0.25f * 1.4426950408889634f : 1.0f;
          v = u32x2{cvtpk(a[0] * sc, a[1] * sc), cvtpk(a[2] * sc, a[3] * sc)};
          u16* dst = (nt == 0 ? ga.o0 : ga.o1) + ((size_t)(head * 64 + (tok >> 5)) * 2 + (g >> 1)) * 256 + (tok & 31) * 8 + (g & 1) * 4;
          *(u32x2*)dst = v;
        } else {
          const int step = (row0 + wm * 32 + mt * 16) >> 4;
          v = u32x2{cvtpk(a[0], a[1]), cvtpk(a[2], a[3])};
          u16* dst = ga.o2 + ((size_t)(head * 128 + step) * 2 + (g & 1)) * 128 + l15 * 8 + (g >> 1) * 4;
          *(u32x2*)dst = v;
        }
      } else if constexpr (MODE == 3 || MODE == 5) {
        u32x2 r = res0[mt];
        if constexpr (MODE == 5) {
          f32x2 st = st_lds[lrow];
          unsigned d0 = cvtpk(lo2f(r[0]) - st[0], hi2f(r[0]) - st[0]), d1 = cvtpk(lo2f(r[1]) - st[0], hi2f(r[1]) - st[0]);
          r[0] = cvtpk(lo2f(d0) * st[1], hi2f(d0) * st[1]);
          r[1] = cvtpk(lo2f(d1) * st[1], hi2f(d1) * st[1]);
        }
        unsigned a0 = cvtpk(a[0], a[1]), a1 = cvtpk(a[2], a[3]);
        v = u32x2{cvtpk(lo2f(r[0]) + lo2f(a0), hi2f(r[0]) + hi2f(a0)), cvtpk(lo2f(r[1]) + lo2f(a1), hi2f(r[1]) + hi2f(a1))};
        float f0 = lo2f(v[0]), f1 = hi2f(v[0]), f2 = lo2f(v[1]), f3 = hi2f(v[1]);
        float s_ = (f0 + f1) + (f2 + f3), q_ = fmaf(f0, f0, f1 * f1) + fmaf(f2, f2, f3 * f3);
        s_ = add_x32(add_x16(s_)); q_ = add_x32(add_x16(q_));
        if (g == 0) *(f32x2*)(ga.pout + (size_t)tok * 64 + (cb * 4 + wn) * 2) = f32x2{s_, q_};
      } else if constexpr (MODE == 4 || MODE == 6) {
        unsigned a0 = cvtpk(a[0], a[1]), a1 = cvtpk(a[2], a[3]);
        v = u32x2{cvtpk(gelu_tanh(lo2f(a0)), gelu_tanh(hi2f(a0))), cvtpk(gelu_tanh(lo2f(a1)), gelu_tanh(hi2f(a1)))};
      } else {
        u32x2 x1 = res0[mt], hh = res1[mt];
        unsigned a0 = cvtpk(a[0], a[1]), a1 = cvtpk(a[2], a[3]);
        unsigned b0 = cvtpk(lo2f(x1[0]) + lo2f(a0), hi2f(x1[0]) + hi2f(a0));
        unsigned b1 = cvtpk(lo2f(x1[1]) + lo2f(a1), hi2f(x1[1]) + hi2f(a1));
        v = u32x2{cvtpk(lo2f(hh[0]) + lo2f(b0), hi2f(hh[0]) + hi2f(b0)), cvtpk(lo2f(hh[1]) + lo2f(b1), hi2f(hh[1]) + hi2f(b1))};
      }
      if constexpr (MODE != 1) *(u32x2*)(ost + lrow * C::RSO + nt * 128 + wn * 32 + g * 8) = v;
    }
  }
  if constexpr (MODE != 1) {
    lds_bar();
#pragma unroll
    for (int j = 0; j < NT; ++j) {
      const int c = tid + 512 * j, row = c / (8 * NT), cc = c % (8 * NT);
      const u32x4 v = *(const u32x4*)(ost + row * C::RSO + cc * 16);
      const int tok = row0 + row;
      if constexpr (MODE == 4) {
        const int grp = cb >> 1;
        const int sh = (grp == 0) ? 1 : (grp == 1) ? -1 : (grp == 2) ? 2 : -2;
        const int col = cb * 64 + cc * 8, dt = tok + sh, src = tok - sh;
        if (dt >= 0 && dt < SEQ) *(u32x4*)(ga.o0 + (size_t)dt * DM + col) = v;
        if (src < 0 || src >= SEQ) *(u32x4*)(ga.o0 + (size_t)tok * DM + col) = u32x4{0u, 0u, 0u, 0u};
      } else if constexpr (MODE == 6) {
        const int col = cb * 192 + cc * 8;
        if (col < FF) *(u32x4*)(ga.o0 + (size_t)tok * FF + col) = v;
      } else {
        *(u32x4*)(ga.o0 + (size_t)tok * DM + cb * 64 + cc * 8) = v;
      }
    }
  }
  STAMP(MODE, 3);
}

constexpr int AT_K = 0, AT_V = 65536, AT_ONES = 131072, AT_Q = AT_ONES + 8192, AT_LDS = AT_Q + 8192;

__global__ __launch_bounds__(512, 2) void attn_kernel(const u16* Qf, const u16* Kf, const u16* Vf, u16* O, const u16* wa,
                                                       const u16* wb, const u16* wc, const u16* wd, const u16* we) {
  extern __shared__ __attribute__((aligned(16))) char lds[];
  STAMP(2, 0);
  const int tid = threadIdx.x, lane = tid & 63;
  const int wid = __builtin_amdgcn_readfirstlane(tid >> 6);
  const int xcd = blockIdx.x & 7, idx = blockIdx.x >> 3;
  const int head = xcd * 4 + (idx >> 3), qb = idx & 7;
  const unsigned lds0 = (unsigned)(uintptr_t)lds;
  const int r = lane & 31, h = lane >> 5;
  const char* kg = (const char*)Kf + (size_t)head * 65536;
  const char* vg = (const char*)Vf + (size_t)head * 65536;
  const char* qg = (const char*)Qf + (size_t)head * 65536 + (qb * 8 + wid) * 1024;
  const unsigned lo = lane * 16;

  glds16(qg, lo, lds0 + AT_Q + wid * 1024);
#pragma unroll
  for (int c = 0; c < 8; ++c) {
    glds16(kg, lo + c * 8192 + wid * 1024, lds0 + AT_K + c * 8192 + wid * 1024);
    glds16(vg, lo + c * 8192 + wid * 1024, lds0 + AT_V + c * 8192 + wid * 1024);
  }
  *(u32x4*)(lds + AT_ONES + tid * 16) = u32x4{0x3F803F80u, 0x3F803F80u, 0x3F803F80u, 0x3F803F80u};

  lds_cptr kp = (lds_cptr)lds + AT_K + lane * 16;
  lds_cptr vp = (lds_cptr)lds + ((r < 16) ? (AT_V + h * 256 + r * 16) : (AT_ONES + (r - 16) * 16));
  const int vstep = (r < 16) ? 4096 : 0;

  f32x16 o, negm, sA, sB;
#pragma unroll
  for (int i = 0; i < 16; ++i) { o[i] = 0.f; negm[i] = 0.f; }

  wait_vm_bar<10>();
  STAMP(2, 1);
  unsigned tw[5];
  {
    unsigned t = (unsigned)(idx * 512 + tid) * 128u;
    unsigned ta = t < 512u * 1024u ? t : 0u, tb = t < (unsigned)(DM * FF * 2) ? t : 0u;
    asm volatile("global_load_dword %0, %5, %7\n\tglobal_load_dword %1, %5, %8\n\tglobal_load_dword %2, %5, %9\n\t"
                 "global_load_dword %3, %6, %10\n\tglobal_load_dword %4, %6, %11"
                 : "=&v"(tw[0]), "=&v"(tw[1]), "=&v"(tw[2]), "=&v"(tw[3]), "=&v"(tw[4])
                 : "v"(ta), "v"(tb), "s"(wa), "s"(wb), "s"(wc), "s"(wd), "s"(we)
                 : "memory");
  }
  const bf16x8 qf = lds_rd128((lds_cptr)lds + AT_Q + wid * 1024 + lane * 16);
  bf16x8 kfA, kfB, vA0, vA1, vB0, vB1;
  u32x4 pA0, pA1, pB0, pB1;
  {
    sA = MFMA32(lds_rd128(kp), qf, negm);
    kfA = lds_rd128(kp + 1024);
    float mx = sA[0];
#pragma unroll
    for (int i = 1; i < 16; ++i) mx = fmaxf(mx, sA[i]);
    const float d = max_x32(mx) + 7.0f;
#pragma unroll
    for (int i = 0; i < 16; ++i) { negm[i] = -d; sA[i] -= d; }
    vB0 = lds_rd128(vp); vB1 = vB0;
    pB0 = u32x4{0u, 0u, 0u, 0u}; pB1 = pB0;
  }

#define SBAR() __builtin_amdgcn_sched_barrier(0)
#define PK(x, i) __builtin_amdgcn_perm(__float_as_uint(x[(i) + 1]), __float_as_uint(x[i]), 0x07060302u)
#define EXPS(lo_, hi_) _Pragma("unroll") for (int i = lo_; i < hi_; ++i) sc[i] = __builtin_amdgcn_exp2f(sc[i])
  auto step = [&](f32x16& sc, f32x16& sn, const bf16x8& kf, bf16x8& kfn, const bf16x8& vp0, const bf16x8& vp1,
                  const u32x4& pp0, const u32x4& pp1, bf16x8& vc0, bf16x8& vc1, u32x4& pc0, u32x4& pc1, lds_cptr kc,
                  lds_cptr vc) {
    sn = MFMA32(kf, qf, negm);
    kfn = lds_rd128(kc + 2048);
    vc0 = lds_rd128(vc); vc1 = lds_rd128(vc + 512);
    SBAR();
    EXPS(0, 5);
    SBAR();
    o = MFMA32(vp0, __builtin_bit_cast(bf16x8, pp0), o);
    SBAR();
    EXPS(5, 11);
    SBAR();
    o = MFMA32(vp1, __builtin_bit_cast(bf16x8, pp1), o);
    SBAR();
    EXPS(11, 16);
    SBAR();
#pragma unroll
    for (int i = 0; i < 4; ++i) { pc0[i] = PK(sc, 2 * i); pc1[i] = PK(sc, 8 + 2 * i); }
    unsigned chk = (pc0[0] | pc0[1] | pc0[2]) | (pc0[3] | pc1[0] | pc1[1]) | (pc1[2] | pc1[3]);
    if (__builtin_expect(__any((chk & 0x40004000u) != 0), 0)) {
      f32x16 s2 = MFMA32(lds_rd128(kc), qf, negm);
      float mx = s2[0];
#pragma unroll
      for (int i = 1; i < 16; ++i) mx = fmaxf(mx, s2[i]);
      const float d = fmaxf(max_x32(mx) + 7.0f, 0.f);
      const float alpha = __builtin_amdgcn_exp2f(-d);
#pragma unroll
      for (int i = 0; i < 16; ++i) { o[i] *= alpha; negm[i] -= d; sn[i] -= d; s2[i] = __builtin_amdgcn_exp2f(s2[i] - d); }
#pragma unroll
      for (int i = 0; i < 4; ++i) { pc0[i] = PK(s2, 2 * i); pc1[i] = PK(s2, 8 + 2 * i); }
    }
    SBAR();
  };
  auto group = [&]() {
    step(sA, sB, kfA, kfB, vB0, vB1, pB0, pB1, vA0, vA1, pA0, pA1, kp, vp);
    step(sB, sA, kfB, kfA, vA0, vA1, pA0, pA1, vB0, vB1, pB0, pB1, kp + 1024, vp + 1024);
    step(sA, sB, kfA, kfB, vB0, vB1, pB0, pB1, vA0, vA1, pA0, pA1, kp + 2048, vp + 2048);
    step(sB, sA, kfB, kfA, vA0, vA1, pA0, pA1, vB0, vB1, pB0, pB1, kp + 3072, vp + 3072);
    kp += 4096;
    vp += vstep;
  };
#pragma unroll 1
  for (int it = 0; it < 4; ++it) group();
  asm volatile("s_waitcnt vmcnt(0) lgkmcnt(0)\n\ts_barrier" : "+v"(tw[0]), "+v"(tw[1]), "+v"(tw[2]), "+v"(tw[3]), "+v"(tw[4])::"memory");
#pragma unroll 1
  for (int it = 4; it < 16; ++it) group();
  o = MFMA32(vB0, __builtin_bit_cast(bf16x8, pB0), o);
  o = MFMA32(vB1, __builtin_bit_cast(bf16x8, pB1), o);

  STAMP(2, 2);
  const float inv = 1.0f / o[8];
  const int q = qb * 256 + wid * 32 + r;
  u16* dst = O + (size_t)q * DM + head * 16 + 4 * h;
  *(u32x2*)dst = u32x2{cvtpk(o[0] * inv, o[1] * inv), cvtpk(o[2] * inv, o[3] * inv)};
  *(u32x2*)(dst + 8) = u32x2{cvtpk(o[4] * inv, o[5] * inv), cvtpk(o[6] * inv, o[7] * inv)};
  STAMP(2, 3);
}

template <int MODE>
static void launch_gemm(const GArgs& ga, hipStream_t stream) {
  (void)hipFuncSetAttribute((const void*)gemm_kernel<MODE>, hipFuncAttributeMaxDynamicSharedMemorySize, Cfg<MODE>::LDS);
  gemm_kernel<MODE><<<256, 512 + 64 * NLD, Cfg<MODE>::LDS, stream>>>(ga);
}

extern "C" void kernel_launch(void* const* d_in, const int* in_sizes, int n_in, void* d_out, int out_size, void* d_ws,
                              size_t ws_size, hipStream_t stream) {
  const u16* x = (const u16*)d_in[0];
  const u16* wqkv = (const u16*)d_in[1];
  const u16* wo = (const u16*)d_in[2];
  const u16* w1 = (const u16*)d_in[4];
  const u16* w2 = (const u16*)d_in[5];
  const u16* w3 = (const u16*)d_in[6];
  const u16* w4 = (const u16*)d_in[7];
  char* ws = (char*)d_ws;
  u16 *Q = (u16*)(ws + WS_Q), *Kf = (u16*)(ws + WS_K), *Vf = (u16*)(ws + WS_V), *O = (u16*)(ws + WS_O),
      *H = (u16*)(ws + WS_H), *T = (u16*)(ws + WS_T), *X1 = (u16*)(ws + WS_X1), *U = (u16*)(ws + WS_U);
  float *PH = (float*)(ws + WS_PH), *PX = (float*)(ws + WS_PX);
  u16* out = (u16*)d_out;

  launch_gemm<1>(GArgs{x, wqkv, nullptr, nullptr, nullptr, nullptr, Q, Kf, Vf}, stream);
  (void)hipFuncSetAttribute((const void*)attn_kernel, hipFuncAttributeMaxDynamicSharedMemorySize, AT_LDS);
  attn_kernel<<<256, 512, AT_LDS, stream>>>(Q, Kf, Vf, O, wo, w1, w2, w3, w4);
  launch_gemm<3>(GArgs{O, wo, x, nullptr, nullptr, PH, H, nullptr, nullptr}, stream);
  launch_gemm<4>(GArgs{H, w1, nullptr, nullptr, PH, nullptr, T, nullptr, nullptr}, stream);
  launch_gemm<5>(GArgs{T, w2, H, nullptr, PH, PX, X1, nullptr, nullptr}, stream);
  launch_gemm<6>(GArgs{X1, w3, nullptr, nullptr, PX, nullptr, U, nullptr, nullptr}, stream);
  launch_gemm<7>(GArgs{U, w4, X1, H, nullptr, nullptr, out, nullptr, nullptr}, stream);
}
```

```cpp
#include <hip/hip_runtime.h>
#include <hip/hip_bf16.h>
#include <stdint.h>

typedef unsigned short u16;
typedef __attribute__((ext_vector_type(8))) short bf16x8;
typedef __attribute__((ext_vector_type(4))) short s16x4;
typedef __attribute__((ext_vector_type(2))) float f32x2;
typedef __attribute__((ext_vector_type(4))) float f32x4;
typedef __attribute__((ext_vector_type(16))) float f32x16;
typedef __attribute__((ext_vector_type(2))) __bf16 bf16x2_t;
typedef __attribute__((ext_vector_type(4))) unsigned u32x4;
typedef __attribute__((ext_vector_type(2))) unsigned u32x2;
typedef short v4i16_t __attribute__((ext_vector_type(4)));
typedef __attribute__((address_space(3))) const char* lds_cptr;

#define DEV __device__ __forceinline__
#define MFMA16(a, b, c) __builtin_amdgcn_mfma_f32_16x16x32_bf16((a), (b), (c), 0, 0, 0)
#define MFMA32(a, b, c) __builtin_amdgcn_mfma_f32_32x32x16_bf16((a), (b), (c), 0, 0, 0)

constexpr int SEQ = 2048, DM = 512, NH = 32, FF = 1376;

constexpr size_t WS_Q = 0, WS_K = 2u << 20, WS_V = 4u << 20, WS_O = 6u << 20, WS_H = 8u << 20, WS_PH = 10u << 20,
                 WS_PX = 11u << 20, WS_T = 12u << 20, WS_X1 = 14u << 20, WS_U = 16u << 20;

DEV unsigned cvtpk(float lo, float hi) {
  f32x2 v = {lo, hi};
  bf16x2_t b = __builtin_convertvector(v, bf16x2_t);
  return __builtin_bit_cast(unsigned, b);
}
DEV float lo2f(unsigned p) { return __uint_as_float(p << 16); }
DEV float hi2f(unsigned p) { return __uint_as_float(p & 0xffff0000u); }
DEV float rbf(float x) { return lo2f(cvtpk(x, x)); }

DEV void glds16(const void* gbase, unsigned voff, unsigned lds_base) {
  unsigned sv;
  asm volatile("s_mov_b32 %0, m0\n\ts_mov_b32 m0, %3\n\ts_nop 0\n\tglobal_load_lds_dwordx4 %1, %2\n\ts_mov_b32 m0, %0"
               : "=&s"(sv)
               : "v"(voff), "s"(gbase), "s"(lds_base)
               : "memory");
}
DEV u32x4 aload4(const void* gbase, unsigned voff) {
  u32x4 r;
  asm volatile("global_load_dwordx4 %0, %1, %2" : "=v"(r) : "v"(voff), "s"(gbase) : "memory");
  return r;
}
DEV u32x2 aload2(const void* gbase, unsigned voff) {
  u32x2 r;
  asm volatile("global_load_dwordx2 %0, %1, %2" : "=v"(r) : "v"(voff), "s"(gbase) : "memory");
  return r;
}
template <class T>
DEV void pin(T& x) { asm volatile("" : "+v"(x)); }
template <int N>
DEV void wait_vm_bar() {
  asm volatile("s_waitcnt vmcnt(%0) lgkmcnt(0)\n\ts_barrier" ::"i"(N) : "memory");
}
DEV void lds_bar() { asm volatile("s_waitcnt lgkmcnt(0)\n\ts_barrier" ::: "memory"); }
DEV bf16x8 lds_rd128(lds_cptr p) { return *(const __attribute__((address_space(3))) bf16x8*)p; }
DEV s16x4 lds_tr(lds_cptr p) {
  return __builtin_bit_cast(s16x4, __builtin_amdgcn_ds_read_tr16_b64_v4i16((__attribute__((address_space(3))) v4i16_t*)p));
}
template <int CTRL>
DEV float dpp_add(float v) {
  int x = __builtin_amdgcn_update_dpp(0, __float_as_int(v), CTRL, 0xf, 0xf, true);
  return v + __int_as_float(x);
}
DEV float add_x16(float v) {
  auto p = __builtin_amdgcn_permlane16_swap(__float_as_uint(v), __float_as_uint(v), false, false);
  return __uint_as_float(p[0]) + __uint_as_float(p[1]);
}
DEV float add_x32(float v) {
  auto p = __builtin_amdgcn_permlane32_swap(__float_as_uint(v), __float_as_uint(v), false, false);
  return __uint_as_float(p[0]) + __uint_as_float(p[1]);
}
DEV float max_x32(float v) {
  auto p = __builtin_amdgcn_permlane32_swap(__float_as_uint(v), __float_as_uint(v), false, false);
  return fmaxf(__uint_as_float(p[0]), __uint_as_float(p[1]));
}
DEV float wave_sum(float v) {
  v = dpp_add<0x121>(v);
  v = dpp_add<0x122>(v);
  v = dpp_add<0x124>(v);
  v = dpp_add<0x128>(v);
  return add_x32(add_x16(v));
}
DEV float dot2_ones(unsigned p, float acc) {
  return __builtin_amdgcn_fdot2_f32_bf16(__builtin_bit_cast(bf16x2_t, p), __builtin_bit_cast(bf16x2_t, 0x3F803F80u), acc, false);
}
DEV float dot2u(unsigned a, unsigned b, float acc) {
  return __builtin_amdgcn_fdot2_f32_bf16(__builtin_bit_cast(bf16x2_t, a), __builtin_bit_cast(bf16x2_t, b), acc, false);
}
DEV float gelu_tanh(float x) {
  const float c = -2.0f * 0.7978845608028654f * 1.4426950408889634f;
  float u = x * (1.0f + 0.044715f * x * x);
  float e = __builtin_amdgcn_exp2f(c * u);
  return x * __builtin_amdgcn_rcpf(1.0f + e);
}
DEV int bswz(int k) { return ((k >> 1) & 1) | (((k >> 3) & 1) << 1); }

#define STAMP(kid, i) do { } while (0)
#define DBG(kid, w, i) do { } while (0)

template <int I, int N, class F>
DEV void static_for(F&& f) {
  if constexpr (I < N) {
    f(std::integral_constant<int, I>{});
    static_for<I + 1, N>(f);
  }
}

DEV void ln_consts(float s, float q, float& mb, float& sd, float& m, float& v) {
  m = s * (1.0f / DM);
  v = fmaxf(q * (1.0f / DM) - m * m, 0.f);
  mb = rbf(m);
  sd = rbf(__builtin_sqrtf(rbf(rbf(v) + 1e-5f)));
}

constexpr int NLD = 4;
template <int MODE>
struct Cfg {
  static constexpr int NT = (MODE == 1 || MODE == 6) ? 3 : 1;
  static constexpr bool LN = (MODE == 1 || MODE == 4 || MODE == 6);
  static constexpr bool PST = (MODE == 4 || MODE == 5 || MODE == 6);
  static constexpr int K = (MODE == 7) ? FF : DM;
  static constexpr int LDA = K;
  static constexpr int LDW = (MODE == 1) ? 3 * DM : (MODE == 6 ? FF : DM);
  static constexpr int NSTAGE = (K + 63) / 64;
  static constexpr int SPB = (NT == 1) ? 2 : 1;
  static constexpr int NB = (NSTAGE + SPB - 1) / SPB;
  static constexpr int RS = 128 * NT;
  static constexpr int BBYTES = 64 * RS;
  static constexpr int SLOT = BBYTES + 8192;
  static constexpr int NGSLOT = 4;
  static constexpr int NSLOT = NGSLOT * SPB;
  static constexpr int DEPTH = NGSLOT - 1;
  static constexpr int X_ST = NSLOT * SLOT;
  static constexpr int X_CS = X_ST + 512;
  static constexpr int X_PX = X_CS + 1536;
  static constexpr int X_OUT = X_PX + 2048;
  static constexpr int RSO = 128 * NT + 16;
  static constexpr int LDS = X_OUT + (MODE == 1 ? 0 : 64 * RSO);
  static constexpr int LPS = 8 * (NT + 1) / NLD;
};

struct GArgs {
  const u16* A;
  const u16* W;
  const u16* r0;
  const u16* r1;
  const float* pin_;
  float* pout;
  u16* o0;
  u16* o1;
  u16* o2;
};

template <int MODE>
__global__ __launch_bounds__(512 + 64 * NLD, 2) void gemm_kernel(GArgs ga) {
  using C = Cfg<MODE>;
  constexpr int NT = C::NT;
  extern __shared__ __attribute__((aligned(16))) char lds[];
  STAMP(MODE, 0);
  const int tid = threadIdx.x, lane = tid & 63;
  const int wid = __builtin_amdgcn_readfirstlane(tid >> 6);
  const int xcd = blockIdx.x & 7, idx = blockIdx.x >> 3;
  const int rb = xcd * 4 + (idx >> 3), cb = idx & 7;
  const int row0 = rb * 64;
  const unsigned lds0 = (unsigned)(uintptr_t)lds;

  if (wid >= 8) {
    const int L = wid - 8;
    constexpr int IA = 8 / NLD, IB = 8 * NT / NLD;
    unsigned boff[IB];
    int bk[IB];
#pragma unroll
    for (int j = 0; j < IB; ++j) {
      int o = (L * IB + j) * 1024 + lane * 16;
      int k = o / C::RS, rem = o % C::RS;
      int grp = rem >> 7, blk = (rem >> 5) & 3, hf = (rem >> 4) & 1;
      int col;
      if (MODE == 1) col = grp * 512 + cb * 64;
      else col = cb * (64 * NT) + grp * 64;
      col += ((blk ^ bswz(k)) * 16) + hf * 8;
      if (MODE == 6 && col > FF - 8) col = FF - 8;
      bk[j] = k;
      boff[j] = (unsigned)(k * C::LDW + col) * 2u;
    }
    unsigned aoffj[IA];
#pragma unroll
    for (int j = 0; j < IA; ++j) {
      int r = (L * IA + j) * 8 + (lane >> 3), c = (lane & 7) ^ ((r >> 1) & 7);
      aoffj[j] = (unsigned)((row0 + r) * C::LDA + c * 8) * 2u;
    }
    auto issue_sub = [&](int s) {
      const unsigned slot = lds0 + (unsigned)((s % C::NSLOT) * C::SLOT);
#pragma unroll
      for (int j = 0; j < IA; ++j) glds16(ga.A, aoffj[j] + s * 128, slot + C::BBYTES + (L * IA + j) * 1024);
#pragma unroll
      for (int j = 0; j < IB; ++j) {
        unsigned off = boff[j] + (unsigned)(s * 64 * C::LDW) * 2u;
        if (MODE == 7 && s == C::NSTAGE - 1 && s * 64 + bk[j] >= FF) off = boff[j] + (unsigned)((FF - 64) * C::LDW) * 2u;
        glds16(ga.W, off, slot + (unsigned)(L * IB + j) * 1024u);
      }
    };
    auto issue_group = [&](auto g_) {
      constexpr int gg = g_.value;
      static_for<0, C::SPB>([&](auto u_) { if constexpr (gg < C::NB && gg * C::SPB + u_.value < C::NSTAGE) issue_sub(gg * C::SPB + u_.value); });
    };
    issue_group(std::integral_constant<int, 0>{});
    issue_group(std::integral_constant<int, 1>{});
    static_for<0, C::NB>([&](auto b_) {
      constexpr int b = b_.value;
      constexpr int lastg0 = (b == 0) ? 1 : b + C::DEPTH - 1;
      constexpr int lastg = lastg0 < C::NB - 1 ? lastg0 : C::NB - 1;
      constexpr int sub_after = ((lastg + 1) * C::SPB > C::NSTAGE ? C::NSTAGE : (lastg + 1) * C::SPB) - ((b + 1) * C::SPB > C::NSTAGE ? C::NSTAGE : (b + 1) * C::SPB);
      asm volatile("s_waitcnt vmcnt(%0)" ::"i"(sub_after * C::LPS) : "memory");
      if (wid == 8) DBG(MODE, 1, b);
      wait_vm_bar<sub_after * C::LPS>();
      if constexpr (b == 0) {
        static_for<2, C::DEPTH + 1>([&](auto g_) { issue_group(g_); });
      } else {
        issue_group(std::integral_constant<int, b + C::DEPTH>{});
      }
    });
    return;
  }

  const int wm = wid & 1, wn = wid >> 1;
  const int l15 = lane & 15, g = lane >> 4;
  u32x4 ps[2];
  u32x2 res0[2], res1[2];
  if constexpr (C::PST) {
    const float* pp = ga.pin_ + (size_t)(row0 + (tid >> 3)) * 64 + (tid & 7) * 8;
    ps[0] = *(const u32x4*)pp;
    ps[1] = *(const u32x4*)(pp + 4);
  }
  if constexpr (MODE == 3 || MODE == 5 || MODE == 7) {
#pragma unroll
    for (int mt = 0; mt < 2; ++mt) {
      size_t o = (size_t)(row0 + wm * 32 + mt * 16 + l15) * DM + cb * 64 + wn * 16 + 4 * g;
      res0[mt] = *(const u32x2*)(ga.r0 + o);
      if constexpr (MODE == 7) res1[mt] = *(const u32x2*)(ga.r1 + o);
    }
  }

  int xoff[2][2];
#pragma unroll
  for (int mt = 0; mt < 2; ++mt)
#pragma unroll
    for (int kk = 0; kk < 2; ++kk) {
      int r = wm * 32 + mt * 16 + l15;
      xoff[mt][kk] = C::BBYTES + r * 128 + (((4 * kk + g) ^ ((r >> 1) & 7)) * 16);
    }
  int woff[2][2];
  {
    int q = l15 >> 2, p = l15 & 3;
#pragma unroll
    for (int kk = 0; kk < 2; ++kk)
#pragma unroll
      for (int t = 0; t < 2; ++t) {
        int k = 32 * kk + 8 * g + 4 * t + q;
        woff[kk][t] = k * C::RS + ((wn ^ bswz(k)) * 32) + p * 8;
      }
  }

  f32x4 acc[2][NT];
  f32x4 cs[NT];
  const bf16x8 ones8 = {0x3F80, 0x3F80, 0x3F80, 0x3F80, 0x3F80, 0x3F80, 0x3F80, 0x3F80};
#pragma unroll
  for (int nt = 0; nt < NT; ++nt) {
    cs[nt] = f32x4{0.f, 0.f, 0.f, 0.f};
#pragma unroll
    for (int mt = 0; mt < 2; ++mt) acc[mt][nt] = f32x4{0.f, 0.f, 0.f, 0.f};
  }
  f32x2* st_lds = (f32x2*)(lds + C::X_ST);

    {
      if constexpr (C::PST) {
        float s_ = (__uint_as_float(ps[0][0]) + __uint_as_float(ps[0][2])) + (__uint_as_float(ps[1][0]) + __uint_as_float(ps[1][2]));
        float q_ = (__uint_as_float(ps[0][1]) + __uint_as_float(ps[0][3])) + (__uint_as_float(ps[1][1]) + __uint_as_float(ps[1][3]));
        s_ = dpp_add<0xB1>(s_); q_ = dpp_add<0xB1>(q_);
        s_ = dpp_add<0x4E>(s_); q_ = dpp_add<0x4E>(q_);
        s_ = dpp_add<0x141>(s_); q_ = dpp_add<0x141>(q_);
        float mb, sd, m, v;
        ln_consts(s_, q_, mb, sd, m, v);
        float M = mb, R = 1.0f / sd;
        if constexpr (MODE == 4) {
          float m2 = (m - mb) * R, v2 = v * R * R;
          float m2b = rbf(m2);
          float sd2 = rbf(__builtin_sqrtf(rbf(rbf(v2) + 1e-5f)));
          M = mb + m2b * sd;
          R = R / sd2;
        }
        if ((tid & 7) == 0) st_lds[tid >> 3] = f32x2{M, R};
      }
    }
  f32x4 sx[2] = {f32x4{0.f, 0.f, 0.f, 0.f}, f32x4{0.f, 0.f, 0.f, 0.f}}, qx[2] = {sx[0], sx[0]};
  static_for<0, C::NB>([&](auto b_) {
    constexpr int s = b_.value;
    if (wid == 0) DBG(MODE, 2, s);
    lds_bar();
    if (wid == 0) DBG(MODE, 0, s);
    if constexpr (s == 0) STAMP(MODE, 1);
    constexpr int S0 = s * C::SPB;
    constexpr int NSUB = ((S0 + C::SPB > C::NSTAGE) ? C::NSTAGE : S0 + C::SPB) - S0;
    constexpr int NKS = 2 * NSUB - ((MODE == 7 && S0 + NSUB == C::NSTAGE) ? 1 : 0);
    bf16x8 xf[NKS][2], wf[NKS][NT];
#pragma unroll
    for (int ks = 0; ks < NKS; ++ks) {
      const lds_cptr bs = (lds_cptr)lds + ((S0 + (ks >> 1)) % C::NSLOT) * C::SLOT;
      const int kk = ks & 1;
#pragma unroll
      for (int nt = 0; nt < NT; ++nt) {
        s16x4 w0 = lds_tr(bs + woff[kk][0] + nt * 128);
        s16x4 w1 = lds_tr(bs + woff[kk][1] + nt * 128);
        wf[ks][nt] = __builtin_shufflevector(w0, w1, 0, 1, 2, 3, 4, 5, 6, 7);
        if (nt == 0) {
#pragma unroll
          for (int mt = 0; mt < 2; ++mt) xf[ks][mt] = lds_rd128(bs + xoff[mt][kk]);
        }
      }
    }
#pragma unroll
    for (int ks = 0; ks < NKS; ++ks) {
#pragma unroll
      for (int nt = 0; nt < NT; ++nt) {
#pragma unroll
        for (int mt = 0; mt < 2; ++mt) {
          if (MODE == 1 && nt == 2) acc[mt][nt] = MFMA16(xf[ks][mt], wf[ks][nt], acc[mt][nt]);
          else acc[mt][nt] = MFMA16(wf[ks][nt], xf[ks][mt], acc[mt][nt]);
        }
      }
    }
    if constexpr (C::LN) {
      auto colsum = [&](auto par_) {
#pragma unroll
        for (int ks = par_.value; ks < NKS; ks += 2)
#pragma unroll
          for (int nt = 0; nt < NT; ++nt) {
            if (MODE == 1 && nt == 2) cs[nt] = MFMA16(ones8, wf[ks][nt], cs[nt]);
            else cs[nt] = MFMA16(wf[ks][nt], ones8, cs[nt]);
          }
      };
      if (wm == 0) colsum(std::integral_constant<int, 0>{});
      else colsum(std::integral_constant<int, 1>{});
    }
    if constexpr (MODE == 1) {
      auto stat = [&](const bf16x8 (&xx)[2]) {
#pragma unroll
        for (int mt = 0; mt < 2; ++mt) {
          sx[mt] = MFMA16(ones8, xx[mt], sx[mt]);
          qx[mt] = MFMA16(xx[mt], xx[mt], qx[mt]);
        }
      };
      if (wn == ((2 * s) & 3)) stat(xf[0]);
      else if (wn == ((2 * s + 1) & 3)) stat(xf[1]);
    }
    __builtin_amdgcn_sched_barrier(0);
  });

  float* cs_lds = (float*)(lds + C::X_CS);
  if constexpr (C::LN) {
#pragma unroll
    for (int nt = 0; nt < NT; ++nt) {
      float* d = cs_lds + ((wm * 4 + wn) * NT + nt) * 16;
      if (MODE == 1 && nt == 2) { if (g == 0) d[l15] = cs[nt][0]; }
      else if (l15 == 0) *(f32x4*)(d + 4 * g) = cs[nt];
    }
    if constexpr (MODE == 1) {
      f32x2* px = (f32x2*)(lds + C::X_PX);
      const int di = l15 & 3;
      float b0 = di == 0 ? qx[0][0] : di == 1 ? qx[0][1] : di == 2 ? qx[0][2] : qx[0][3];
      float b1 = di == 0 ? qx[1][0] : di == 1 ? qx[1][1] : di == 2 ? qx[1][2] : qx[1][3];
      if (g == (l15 >> 2)) { px[wn * 64 + wm * 32 + l15] = f32x2{sx[0][0], b0}; px[wn * 64 + wm * 32 + 16 + l15] = f32x2{sx[1][0], b1}; }
      lds_bar();
      if (tid < 64) {
        f32x2 t = (px[tid] + px[64 + tid]) + (px[128 + tid] + px[192 + tid]);
        float mb, sd, m, v;
        ln_consts(t[0], t[1], mb, sd, m, v);
        st_lds[tid] = f32x2{mb, 1.0f / sd};
      }
    }
    lds_bar();
#pragma unroll
    for (int nt = 0; nt < NT; ++nt) {
      const float* d = cs_lds + (((1 - wm) * 4 + wn) * NT + nt) * 16;
      if (MODE == 1 && nt == 2) cs[nt][0] += d[l15];
      else cs[nt] += *(const f32x4*)(d + 4 * g);
    }
  }
  STAMP(MODE, 2);
  char* ost = lds + C::X_OUT;
#pragma unroll
  for (int mt = 0; mt < 2; ++mt) {
    const int lrow = wm * 32 + mt * 16 + l15;
    const int tok = row0 + lrow;
#pragma unroll
    for (int nt = 0; nt < NT; ++nt) {
      f32x4 a = acc[mt][nt];
      if constexpr (C::LN) {
        if (MODE == 1 && nt == 2) {
          float c = cs[nt][0];
#pragma unroll
          for (int i = 0; i < 4; ++i) {
            f32x2 st = st_lds[wm * 32 + mt * 16 + 4 * g + i];
            a[i] = (a[i] - st[0] * c) * st[1];
          }
        } else {
          f32x4 c = cs[nt];
          f32x2 st = st_lds[lrow];
#pragma unroll
          for (int i = 0; i < 4; ++i) a[i] = (a[i] - st[0] * c[i]) * st[1];
        }
      }
      u32x2 v;
      if constexpr (MODE == 1) {
        const int head = cb * 4 + wn;
        if (nt < 2) {
          float sc = (nt == 0) ? 0.25f * 1.4426950408889634f : 1.0f;
          v = u32x2{cvtpk(a[0] * sc, a[1] * sc), cvtpk(a[2] * sc, a[3] * sc)};
          u16* dst = (nt == 0 ? ga.o0 : ga.o1) + ((size_t)(head * 64 + (tok >> 5)) * 2 + (g >> 1)) * 256 + (tok & 31) * 8 + (g & 1) * 4;
          *(u32x2*)dst = v;
        } else {
          const int step = (row0 + wm * 32 + mt * 16) >> 4;
          v = u32x2{cvtpk(a[0], a[1]), cvtpk(a[2], a[3])};
          u16* dst = ga.o2 + ((size_t)(head * 128 + step) * 2 + (g & 1)) * 128 + l15 * 8 + (g >> 1) * 4;
          *(u32x2*)dst = v;
        }
      } else if constexpr (MODE == 3 || MODE == 5) {
        u32x2 r = res0[mt];
        if constexpr (MODE == 5) {
          f32x2 st = st_lds[lrow];
          unsigned d0 = cvtpk(lo2f(r[0]) - st[0], hi2f(r[0]) - st[0]), d1 = cvtpk(lo2f(r[1]) - st[0], hi2f(r[1]) - st[0]);
          r[0] = cvtpk(lo2f(d0) * st[1], hi2f(d0) * st[1]);
          r[1] = cvtpk(lo2f(d1) * st[1], hi2f(d1) * st[1]);
        }
        unsigned a0 = cvtpk(a[0], a[1]), a1 = cvtpk(a[2], a[3]);
        v = u32x2{cvtpk(lo2f(r[0]) + lo2f(a0), hi2f(r[0]) + hi2f(a0)), cvtpk(lo2f(r[1]) + lo2f(a1), hi2f(r[1]) + hi2f(a1))};
        float f0 = lo2f(v[0]), f1 = hi2f(v[0]), f2 = lo2f(v[1]), f3 = hi2f(v[1]);
        float s_ = (f0 + f1) + (f2 + f3), q_ = fmaf(f0, f0, f1 * f1) + fmaf(f2, f2, f3 * f3);
        s_ = add_x32(add_x16(s_)); q_ = add_x32(add_x16(q_));
        if (g == 0) *(f32x2*)(ga.pout + (size_t)tok * 64 + (cb * 4 + wn) * 2) = f32x2{s_, q_};
      } else if constexpr (MODE == 4 || MODE == 6) {
        unsigned a0 = cvtpk(a[0], a[1]), a1 = cvtpk(a[2], a[3]);
        v = u32x2{cvtpk(gelu_tanh(lo2f(a0)), gelu_tanh(hi2f(a0))), cvtpk(gelu_tanh(lo2f(a1)), gelu_tanh(hi2f(a1)))};
      } else {
        u32x2 x1 = res0[mt], hh = res1[mt];
        unsigned a0 = cvtpk(a[0], a[1]), a1 = cvtpk(a[2], a[3]);
        unsigned b0 = cvtpk(lo2f(x1[0]) + lo2f(a0), hi2f(x1[0]) + hi2f(a0));
        unsigned b1 = cvtpk(lo2f(x1[1]) + lo2f(a1), hi2f(x1[1]) + hi2f(a1));
        v = u32x2{cvtpk(lo2f(hh[0]) + lo2f(b0), hi2f(hh[0]) + hi2f(b0)), cvtpk(lo2f(hh[1]) + lo2f(b1), hi2f(hh[1]) + hi2f(b1))};
      }
      if constexpr (MODE != 1) *(u32x2*)(ost + lrow * C::RSO + nt * 128 + wn * 32 + g * 8) = v;
    }
  }
  if constexpr (MODE != 1) {
    lds_bar();
#pragma unroll
    for (int j = 0; j < NT; ++j) {
      const int c = tid + 512 * j, row = c / (8 * NT), cc = c % (8 * NT);
      const u32x4 v = *(const u32x4*)(ost + row * C::RSO + cc * 16);
      const int tok = row0 + row;
      if constexpr (MODE == 4) {
        const int grp = cb >> 1;
        const int sh = (grp == 0) ? 1 : (grp == 1) ? -1 : (grp == 2) ? 2 : -2;
        const int col = cb * 64 + cc * 8, dt = tok + sh, src = tok - sh;
        if (dt >= 0 && dt < SEQ) *(u32x4*)(ga.o0 + (size_t)dt * DM + col) = v;
        if (src < 0 || src >= SEQ) *(u32x4*)(ga.o0 + (size_t)tok * DM + col) = u32x4{0u, 0u, 0u, 0u};
      } else if constexpr (MODE == 6) {
        const int col = cb * 192 + cc * 8;
        if (col < FF) *(u32x4*)(ga.o0 + (size_t)tok * FF + col) = v;
      } else {
        *(u32x4*)(ga.o0 + (size_t)tok * DM + cb * 64 + cc * 8) = v;
      }
    }
  }
  STAMP(MODE, 3);
}

constexpr int AT_K = 0, AT_V = 65536, AT_ONES = 131072, AT_Q = AT_ONES + 8192, AT_LDS = AT_Q + 8192;

__global__ __launch_bounds__(512, 2) void attn_kernel(const u16* Qf, const u16* Kf, const u16* Vf, u16* O, const u16* wa,
                                                       const u16* wb, const u16* wc, const u16* wd, const u16* we) {
  extern __shared__ __attribute__((aligned(16))) char lds[];
  STAMP(2, 0);
  const int tid = threadIdx.x, lane = tid & 63;
  const int wid = __builtin_amdgcn_readfirstlane(tid >> 6);
  const int xcd = blockIdx.x & 7, idx = blockIdx.x >> 3;
  const int head = xcd * 4 + (idx >> 3), qb = idx & 7;
  const unsigned lds0 = (unsigned)(uintptr_t)lds;
  const int r = lane & 31, h = lane >> 5;
  const char* kg = (const char*)Kf + (size_t)head * 65536;
  const char* vg = (const char*)Vf + (size_t)head * 65536;
  const char* qg = (const char*)Qf + (size_t)head * 65536 + (qb * 8 + wid) * 1024;
  const unsigned lo = lane * 16;

  glds16(qg, lo, lds0 + AT_Q + wid * 1024);
#pragma unroll
  for (int c = 0; c < 2; ++c) {
    glds16(kg, lo + c * 8192 + wid * 1024, lds0 + AT_K + c * 8192 + wid * 1024);
    glds16(vg, lo + c * 8192 + wid * 1024, lds0 + AT_V + c * 8192 + wid * 1024);
  }
  *(u32x4*)(lds + AT_ONES + tid * 16) = u32x4{0x3F803F80u, 0x3F803F80u, 0x3F803F80u, 0x3F803F80u};

  lds_cptr kp = (lds_cptr)lds + AT_K + lane * 16;
  lds_cptr vp = (lds_cptr)lds + ((r < 16) ? (AT_V + h * 256 + r * 16) : (AT_ONES + (r - 16) * 16));
  const int vstep = (r < 16) ? 4096 : 0;

  f32x16 o, negm, sA, sB;
#pragma unroll
  for (int i = 0; i < 16; ++i) { o[i] = 0.f; negm[i] = 0.f; }

  wait_vm_bar<0>();
  STAMP(2, 1);
#pragma unroll
  for (int c = 2; c < 8; ++c) {
    glds16(kg, lo + c * 8192 + wid * 1024, lds0 + AT_K + c * 8192 + wid * 1024);
    glds16(vg, lo + c * 8192 + wid * 1024, lds0 + AT_V + c * 8192 + wid * 1024);
  }
  unsigned tw[5];
  {
    unsigned t = (unsigned)(idx * 512 + tid) * 128u;
    unsigned ta = t < 512u * 1024u ? t : 0u, tb = t < (unsigned)(DM * FF * 2) ? t : 0u;
    asm volatile("global_load_dword %0, %5, %7\n\tglobal_load_dword %1, %5, %8\n\tglobal_load_dword %2, %5, %9\n\t"
                 "global_load_dword %3, %6, %10\n\tglobal_load_dword %4, %6, %11"
                 : "=&v"(tw[0]), "=&v"(tw[1]), "=&v"(tw[2]), "=&v"(tw[3]), "=&v"(tw[4])
                 : "v"(ta), "v"(tb), "s"(wa), "s"(wb), "s"(wc), "s"(wd), "s"(we)
                 : "memory");
  }
  const bf16x8 qf = lds_rd128((lds_cptr)lds + AT_Q + wid * 1024 + lane * 16);
  bf16x8 kfA, kfB, vA0, vA1, vB0, vB1;
  u32x4 pA0, pA1, pB0, pB1;
  {
    sA = MFMA32(lds_rd128(kp), qf, negm);
    kfA = lds_rd128(kp + 1024);
    float mx = sA[0];
#pragma unroll
    for (int i = 1; i < 16; ++i) mx = fmaxf(mx, sA[i]);
    const float d = max_x32(mx) + 7.0f;
#pragma unroll
    for (int i = 0; i < 16; ++i) { negm[i] = -d; sA[i] -= d; }
    vB0 = lds_rd128(vp); vB1 = vB0;
    pB0 = u32x4{0u, 0u, 0u, 0u}; pB1 = pB0;
  }

#define SBAR() __builtin_amdgcn_sched_barrier(0)
#define PK(x, i) __builtin_amdgcn_perm(__float_as_uint(x[(i) + 1]), __float_as_uint(x[i]), 0x07060302u)
#define EXPS(lo_, hi_) _Pragma("unroll") for (int i = lo_; i < hi_; ++i) sc[i] = __builtin_amdgcn_exp2f(sc[i])
  auto step = [&](f32x16& sc, f32x16& sn, const bf16x8& kf, bf16x8& kfn, const bf16x8& vp0, const bf16x8& vp1,
                  const u32x4& pp0, const u32x4& pp1, bf16x8& vc0, bf16x8& vc1, u32x4& pc0, u32x4& pc1, lds_cptr kc,
                  lds_cptr vc) {
    sn = MFMA32(kf, qf, negm);
    kfn = lds_rd128(kc + 2048);
    vc0 = lds_rd128(vc); vc1 = lds_rd128(vc + 512);
    SBAR();
    EXPS(0, 5);
    SBAR();
    o = MFMA32(vp0, __builtin_bit_cast(bf16x8, pp0), o);
    SBAR();
    EXPS(5, 11);
    SBAR();
    o = MFMA32(vp1, __builtin_bit_cast(bf16x8, pp1), o);
    SBAR();
    EXPS(11, 16);
    SBAR();
#pragma unroll
    for (int i = 0; i < 4; ++i) { pc0[i] = PK(sc, 2 * i); pc1[i] = PK(sc, 8 + 2 * i); }
    unsigned chk = (pc0[0] | pc0[1] | pc0[2]) | (pc0[3] | pc1[0] | pc1[1]) | (pc1[2] | pc1[3]);
    if (__builtin_expect(__any((chk & 0x40004000u) != 0), 0)) {
      f32x16 s2 = MFMA32(lds_rd128(kc), qf, negm);
      float mx = s2[0];
#pragma unroll
      for (int i = 1; i < 16; ++i) mx = fmaxf(mx, s2[i]);
      const float d = fmaxf(max_x32(mx) + 7.0f, 0.f);
      const float alpha = __builtin_amdgcn_exp2f(-d);
#pragma unroll
      for (int i = 0; i < 16; ++i) { o[i] *= alpha; negm[i] -= d; sn[i] -= d; s2[i] = __builtin_amdgcn_exp2f(s2[i] - d); }
#pragma unroll
      for (int i = 0; i < 4; ++i) { pc0[i] = PK(s2, 2 * i); pc1[i] = PK(s2, 8 + 2 * i); }
    }
    SBAR();
  };
  auto group = [&]() {
    step(sA, sB, kfA, kfB, vB0, vB1, pB0, pB1, vA0, vA1, pA0, pA1, kp, vp);
    step(sB, sA, kfB, kfA, vA0, vA1, pA0, pA1, vB0, vB1, pB0, pB1, kp + 1024, vp + 1024);
    step(sA, sB, kfA, kfB, vB0, vB1, pB0, pB1, vA0, vA1, pA0, pA1, kp + 2048, vp + 2048);
    step(sB, sA, kfB, kfA, vA0, vA1, pA0, pA1, vB0, vB1, pB0, pB1, kp + 3072, vp + 3072);
    kp += 4096;
    vp += vstep;
  };
#pragma unroll 1
  for (int it = 0; it < 3; ++it) group();
  asm volatile("s_waitcnt vmcnt(0) lgkmcnt(0)\n\ts_barrier" : "+v"(tw[0]), "+v"(tw[1]), "+v"(tw[2]), "+v"(tw[3]), "+v"(tw[4])::"memory");
#pragma unroll 1
  for (int it = 3; it < 16; ++it) group();
  o = MFMA32(vB0, __builtin_bit_cast(bf16x8, pB0), o);
  o = MFMA32(vB1, __builtin_bit_cast(bf16x8, pB1), o);

  STAMP(2, 2);
  const float inv = 1.0f / o[8];
  const int q = qb * 256 + wid * 32 + r;
  u16* dst = O + (size_t)q * DM + head * 16 + 4 * h;
  *(u32x2*)dst = u32x2{cvtpk(o[0] * inv, o[1] * inv), cvtpk(o[2] * inv, o[3] * inv)};
  *(u32x2*)(dst + 8) = u32x2{cvtpk(o[4] * inv, o[5] * inv), cvtpk(o[6] * inv, o[7] * inv)};
  STAMP(2, 3);
}

template <int MODE>
static void launch_gemm(const GArgs& ga, hipStream_t stream) {
  (void)hipFuncSetAttribute((const void*)gemm_kernel<MODE>, hipFuncAttributeMaxDynamicSharedMemorySize, Cfg<MODE>::LDS);
  gemm_kernel<MODE><<<256, 512 + 64 * NLD, Cfg<MODE>::LDS, stream>>>(ga);
}

extern "C" void kernel_launch(void* const* d_in, const int* in_sizes, int n_in, void* d_out, int out_size, void* d_ws,
                              size_t ws_size, hipStream_t stream) {
  const u16* x = (const u16*)d_in[0];
  const u16* wqkv = (const u16*)d_in[1];
  const u16* wo = (const u16*)d_in[2];
  const u16* w1 = (const u16*)d_in[4];
  const u16* w2 = (const u16*)d_in[5];
  const u16* w3 = (const u16*)d_in[6];
  const u16* w4 = (const u16*)d_in[7];
  char* ws = (char*)d_ws;
  u16 *Q = (u16*)(ws + WS_Q), *Kf = (u16*)(ws + WS_K), *Vf = (u16*)(ws + WS_V), *O = (u16*)(ws + WS_O),
      *H = (u16*)(ws + WS_H), *T = (u16*)(ws + WS_T), *X1 = (u16*)(ws + WS_X1), *U = (u16*)(ws + WS_U);
  float *PH = (float*)(ws + WS_PH), *PX = (float*)(ws + WS_PX);
  u16* out = (u16*)d_out;

  launch_gemm<1>(GArgs{x, wqkv, nullptr, nullptr, nullptr, nullptr, Q, Kf, Vf}, stream);
  (void)hipFuncSetAttribute((const void*)attn_kernel, hipFuncAttributeMaxDynamicSharedMemorySize, AT_LDS);
  attn_kernel<<<256, 512, AT_LDS, stream>>>(Q, Kf, Vf, O, wo, w1, w2, w3, w4);
  launch_gemm<3>(GArgs{O, wo, x, nullptr, nullptr, PH, H, nullptr, nullptr}, stream);
  launch_gemm<4>(GArgs{H, w1, nullptr, nullptr, PH, nullptr, T, nullptr, nullptr}, stream);
  launch_gemm<5>(GArgs{T, w2, H, nullptr, PH, PX, X1, nullptr, nullptr}, stream);
  launch_gemm<6>(GArgs{X1, w3, nullptr, nullptr, PX, nullptr, U, nullptr, nullptr}, stream);
  launch_gemm<7>(GArgs{U, w4, X1, H, nullptr, nullptr, out, nullptr, nullptr}, stream);
}
```

```cpp
#include <hip/hip_runtime.h>
#include <hip/hip_bf16.h>
#include <stdint.h>

typedef unsigned short u16;
typedef __attribute__((ext_vector_type(8))) short bf16x8;
typedef __attribute__((ext_vector_type(4))) short s16x4;
typedef __attribute__((ext_vector_type(2))) float f32x2;
typedef __attribute__((ext_vector_type(4))) float f32x4;
typedef __attribute__((ext_vector_type(16))) float f32x16;
typedef __attribute__((ext_vector_type(2))) __bf16 bf16x2_t;
typedef __attribute__((ext_vector_type(4))) unsigned u32x4;
typedef __attribute__((ext_vector_type(2))) unsigned u32x2;
typedef short v4i16_t __attribute__((ext_vector_type(4)));
typedef __attribute__((address_space(3))) const char* lds_cptr;

#define DEV __device__ __forceinline__
#define MFMA16(a, b, c) __builtin_amdgcn_mfma_f32_16x16x32_bf16((a), (b), (c), 0, 0, 0)
#define MFMA32(a, b, c) __builtin_amdgcn_mfma_f32_32x32x16_bf16((a), (b), (c), 0, 0, 0)

constexpr int SEQ = 2048, DM = 512, NH = 32, FF = 1376;

constexpr size_t WS_Q = 0, WS_K = 2u << 20, WS_V = 4u << 20, WS_O = 6u << 20, WS_H = 8u << 20, WS_PH = 10u << 20,
                 WS_PX = 11u << 20, WS_T = 12u << 20, WS_X1 = 14u << 20, WS_U = 16u << 20;

DEV unsigned cvtpk(float lo, float hi) {
  f32x2 v = {lo, hi};
  bf16x2_t b = __builtin_convertvector(v, bf16x2_t);
  return __builtin_bit_cast(unsigned, b);
}
DEV float lo2f(unsigned p) { return __uint_as_float(p << 16); }
DEV float hi2f(unsigned p) { return __uint_as_float(p & 0xffff0000u); }
DEV float rbf(float x) { return lo2f(cvtpk(x, x)); }

DEV void glds16(const void* gbase, unsigned voff, unsigned lds_base) {
  unsigned sv;
  asm volatile("s_mov_b32 %0, m0\n\ts_mov_b32 m0, %3\n\ts_nop 0\n\tglobal_load_lds_dwordx4 %1, %2\n\ts_mov_b32 m0, %0"
               : "=&s"(sv)
               : "v"(voff), "s"(gbase), "s"(lds_base)
               : "memory");
}
DEV u32x4 aload4(const void* gbase, unsigned voff) {
  u32x4 r;
  asm volatile("global_load_dwordx4 %0, %1, %2" : "=v"(r) : "v"(voff), "s"(gbase) : "memory");
  return r;
}
DEV u32x2 aload2(const void* gbase, unsigned voff) {
  u32x2 r;
  asm volatile("global_load_dwordx2 %0, %1, %2" : "=v"(r) : "v"(voff), "s"(gbase) : "memory");
  return r;
}
template <class T>
DEV void pin(T& x) { asm volatile("" : "+v"(x)); }
template <int N>
DEV void wait_vm_bar() {
  asm volatile("s_waitcnt vmcnt(%0) lgkmcnt(0)\n\ts_barrier" ::"i"(N) : "memory");
}
DEV void lds_bar() { asm volatile("s_waitcnt lgkmcnt(0)\n\ts_barrier" ::: "memory"); }
DEV bf16x8 lds_rd128(lds_cptr p) { return *(const __attribute__((address_space(3))) bf16x8*)p; }
DEV s16x4 lds_tr(lds_cptr p) {
  return __builtin_bit_cast(s16x4, __builtin_amdgcn_ds_read_tr16_b64_v4i16((__attribute__((address_space(3))) v4i16_t*)p));
}
template <int CTRL>
DEV float dpp_add(float v) {
  int x = __builtin_amdgcn_update_dpp(0, __float_as_int(v), CTRL, 0xf, 0xf, true);
  return v + __int_as_float(x);
}
DEV float add_x16(float v) {
  auto p = __builtin_amdgcn_permlane16_swap(__float_as_uint(v), __float_as_uint(v), false, false);
  return __uint_as_float(p[0]) + __uint_as_float(p[1]);
}
DEV float add_x32(float v) {
  auto p = __builtin_amdgcn_permlane32_swap(__float_as_uint(v), __float_as_uint(v), false, false);
  return __uint_as_float(p[0]) + __uint_as_float(p[1]);
}
DEV float max_x32(float v) {
  auto p = __builtin_amdgcn_permlane32_swap(__float_as_uint(v), __float_as_uint(v), false, false);
  return fmaxf(__uint_as_float(p[0]), __uint_as_float(p[1]));
}
DEV float wave_sum(float v) {
  v = dpp_add<0x121>(v);
  v = dpp_add<0x122>(v);
  v = dpp_add<0x124>(v);
  v = dpp_add<0x128>(v);
  return add_x32(add_x16(v));
}
DEV float dot2_ones(unsigned p, float acc) {
  return __builtin_amdgcn_fdot2_f32_bf16(__builtin_bit_cast(bf16x2_t, p), __builtin_bit_cast(bf16x2_t, 0x3F803F80u), acc, false);
}
DEV float dot2u(unsigned a, unsigned b, float acc) {
  return __builtin_amdgcn_fdot2_f32_bf16(__builtin_bit_cast(bf16x2_t, a), __builtin_bit_cast(bf16x2_t, b), acc, false);
}
DEV float gelu_tanh(float x) {
  const float c = -2.0f * 0.7978845608028654f * 1.4426950408889634f;
  float u = x * (1.0f + 0.044715f * x * x);
  float e = __builtin_amdgcn_exp2f(c * u);
  return x * __builtin_amdgcn_rcpf(1.0f + e);
}
DEV int bswz(int k) { return ((k >> 1) & 1) | (((k >> 3) & 1) << 1); }

#define STAMP(kid, i) do { } while (0)
#define DBG(kid, w, i) do { } while (0)

template <int I, int N, class F>
DEV void static_for(F&& f) {
  if constexpr (I < N) {
    f(std::integral_constant<int, I>{});
    static_for<I + 1, N>(f);
  }
}

DEV void ln_consts(float s, float q, float& mb, float& sd, float& m, float& v) {
  m = s * (1.0f / DM);
  v = fmaxf(q * (1.0f / DM) - m * m, 0.f);
  mb = rbf(m);
  sd = rbf(__builtin_sqrtf(rbf(rbf(v) + 1e-5f)));
}

constexpr int NLD = 4;
template <int MODE>
struct Cfg {
  static constexpr int NT = (MODE == 1 || MODE == 6) ? 3 : 1;
  static constexpr bool LN = (MODE == 1 || MODE == 4 || MODE == 6);
  static constexpr bool PST = (MODE == 4 || MODE == 5 || MODE == 6);
  static constexpr int K = (MODE == 7) ? FF : DM;
  static constexpr int LDA = K;
  static constexpr int LDW = (MODE == 1) ? 3 * DM : (MODE == 6 ? FF : DM);
  static constexpr int NSTAGE = (K + 63) / 64;
  static constexpr int SPB = (NT == 1) ? 2 : 1;
  static constexpr int NB = (NSTAGE + SPB - 1) / SPB;
  static constexpr int RS = 128 * NT;
  static constexpr int BBYTES = 64 * RS;
  static constexpr int SLOT = BBYTES + 8192;
  static constexpr int NGSLOT = 4;
  static constexpr int NSLOT = NGSLOT * SPB;
  static constexpr int DEPTH = NGSLOT - 1;
  static constexpr int X_ST = NSLOT * SLOT;
  static constexpr int X_CS = X_ST + 512;
  static constexpr int X_PX = X_CS + 1536;
  static constexpr int X_OUT = X_PX + 2048;
  static constexpr int RSO = 128 * NT + 16;
  static constexpr int LDS = X_OUT + (MODE == 1 ? 0 : 64 * RSO);
  static constexpr int LPS = 8 * (NT + 1) / NLD;
};

struct GArgs {
  const u16* A;
  const u16* W;
  const u16* r0;
  const u16* r1;
  const float* pin_;
  float* pout;
  u16* o0;
  u16* o1;
  u16* o2;
};

template <int MODE>
__global__ __launch_bounds__(512 + 64 * NLD, 2) void gemm_kernel(GArgs ga) {
  using C = Cfg<MODE>;
  constexpr int NT = C::NT;
  extern __shared__ __attribute__((aligned(16))) char lds[];
  STAMP(MODE, 0);
  const int tid = threadIdx.x, lane = tid & 63;
  const int wid = __builtin_amdgcn_readfirstlane(tid >> 6);
  const int xcd = blockIdx.x & 7, idx = blockIdx.x >> 3;
  const int rb = xcd * 4 + (idx >> 3), cb = idx & 7;
  const int row0 = rb * 64;
  const unsigned lds0 = (unsigned)(uintptr_t)lds;

  if (wid >= 8) {
    const int L = wid - 8;
    constexpr int IA = 8 / NLD, IB = 8 * NT / NLD;
    unsigned boff[IB];
    int bk[IB];
#pragma unroll
    for (int j = 0; j < IB; ++j) {
      int o = (L * IB + j) * 1024 + lane * 16;
      int k = o / C::RS, rem = o % C::RS;
      int grp = rem >> 7, blk = (rem >> 5) & 3, hf = (rem >> 4) & 1;
      int col;
      if (MODE == 1) col = grp * 512 + cb * 64;
      else col = cb * (64 * NT) + grp * 64;
      col += ((blk ^ bswz(k)) * 16) + hf * 8;
      if (MODE == 6 && col > FF - 8) col = FF - 8;
      bk[j] = k;
      boff[j] = (unsigned)(k * C::LDW + col) * 2u;
    }
    unsigned aoffj[IA];
#pragma unroll
    for (int j = 0; j < IA; ++j) {
      int r = (L * IA + j) * 8 + (lane >> 3), c = (lane & 7) ^ ((r >> 1) & 7);
      aoffj[j] = (unsigned)((row0 + r) * C::LDA + c * 8) * 2u;
    }
    auto issue_sub = [&](int s) {
      const unsigned slot = lds0 + (unsigned)((s % C::NSLOT) * C::SLOT);
#pragma unroll
      for (int j = 0; j < IA; ++j) glds16(ga.A, aoffj[j] + s * 128, slot + C::BBYTES + (L * IA + j) * 1024);
#pragma unroll
      for (int j = 0; j < IB; ++j) {
        unsigned off = boff[j] + (unsigned)(s * 64 * C::LDW) * 2u;
        if (MODE == 7 && s == C::NSTAGE - 1 && s * 64 + bk[j] >= FF) off = boff[j] + (unsigned)((FF - 64) * C::LDW) * 2u;
        glds16(ga.W, off, slot + (unsigned)(L * IB + j) * 1024u);
      }
    };
    auto issue_group = [&](auto g_) {
      constexpr int gg = g_.value;
      static_for<0, C::SPB>([&](auto u_) { if constexpr (gg < C::NB && gg * C::SPB + u_.value < C::NSTAGE) issue_sub(gg * C::SPB + u_.value); });
    };
    issue_group(std::integral_constant<int, 0>{});
    issue_group(std::integral_constant<int, 1>{});
    static_for<0, C::NB>([&](auto b_) {
      constexpr int b = b_.value;
      constexpr int lastg0 = (b == 0) ? 1 : b + C::DEPTH - 1;
      constexpr int lastg = lastg0 < C::NB - 1 ? lastg0 : C::NB - 1;
      constexpr int sub_after = ((lastg + 1) * C::SPB > C::NSTAGE ? C::NSTAGE : (lastg + 1) * C::SPB) - ((b + 1) * C::SPB > C::NSTAGE ? C::NSTAGE : (b + 1) * C::SPB);
      asm volatile("s_waitcnt vmcnt(%0)" ::"i"(sub_after * C::LPS) : "memory");
      if (wid == 8) DBG(MODE, 1, b);
      wait_vm_bar<sub_after * C::LPS>();
      if constexpr (b == 0) {
        static_for<2, C::DEPTH + 1>([&](auto g_) { issue_group(g_); });
      } else {
        issue_group(std::integral_constant<int, b + C::DEPTH>{});
      }
    });
    return;
  }

  const int wm = wid & 1, wn = wid >> 1;
  const int l15 = lane & 15, g = lane >> 4;
  u32x4 ps[2];
  u32x2 res0[2], res1[2];
  if constexpr (C::PST) {
    const float* pp = ga.pin_ + (size_t)(row0 + (tid >> 3)) * 64 + (tid & 7) * 8;
    ps[0] = *(const u32x4*)pp;
    ps[1] = *(const u32x4*)(pp + 4);
  }
  if constexpr (MODE == 3 || MODE == 5 || MODE == 7) {
#pragma unroll
    for (int mt = 0; mt < 2; ++mt) {
      size_t o = (size_t)(row0 + wm * 32 + mt * 16 + l15) * DM + cb * 64 + wn * 16 + 4 * g;
      res0[mt] = *(const u32x2*)(ga.r0 + o);
      if constexpr (MODE == 7) res1[mt] = *(const u32x2*)(ga.r1 + o);
    }
  }

  int xoff[2][2];
#pragma unroll
  for (int mt = 0; mt < 2; ++mt)
#pragma unroll
    for (int kk = 0; kk < 2; ++kk) {
      int r = wm * 32 + mt * 16 + l15;
      xoff[mt][kk] = C::BBYTES + r * 128 + (((4 * kk + g) ^ ((r >> 1) & 7)) * 16);
    }
  int woff[2][2];
  {
    int q = l15 >> 2, p = l15 & 3;
#pragma unroll
    for (int kk = 0; kk < 2; ++kk)
#pragma unroll
      for (int t = 0; t < 2; ++t) {
        int k = 32 * kk + 8 * g + 4 * t + q;
        woff[kk][t] = k * C::RS + ((wn ^ bswz(k)) * 32) + p * 8;
      }
  }

  f32x4 acc[2][NT];
  f32x4 cs[NT];
  const bf16x8 ones8 = {0x3F80, 0x3F80, 0x3F80, 0x3F80, 0x3F80, 0x3F80, 0x3F80, 0x3F80};
#pragma unroll
  for (int nt = 0; nt < NT; ++nt) {
    cs[nt] = f32x4{0.f, 0.f, 0.f, 0.f};
#pragma unroll
    for (int mt = 0; mt < 2; ++mt) acc[mt][nt] = f32x4{0.f, 0.f, 0.f, 0.f};
  }
  f32x2* st_lds = (f32x2*)(lds + C::X_ST);

    {
      if constexpr (C::PST) {
        float s_ = (__uint_as_float(ps[0][0]) + __uint_as_float(ps[0][2])) + (__uint_as_float(ps[1][0]) + __uint_as_float(ps[1][2]));
        float q_ = (__uint_as_float(ps[0][1]) + __uint_as_float(ps[0][3])) + (__uint_as_float(ps[1][1]) + __uint_as_float(ps[1][3]));
        s_ = dpp_add<0xB1>(s_); q_ = dpp_add<0xB1>(q_);
        s_ = dpp_add<0x4E>(s_); q_ = dpp_add<0x4E>(q_);
        s_ = dpp_add<0x141>(s_); q_ = dpp_add<0x141>(q_);
        float mb, sd, m, v;
        ln_consts(s_, q_, mb, sd, m, v);
        float M = mb, R = 1.0f / sd;
        if constexpr (MODE == 4) {
          float m2 = (m - mb) * R, v2 = v * R * R;
          float m2b = rbf(m2);
          float sd2 = rbf(__builtin_sqrtf(rbf(rbf(v2) + 1e-5f)));
          M = mb + m2b * sd;
          R = R / sd2;
        }
        if ((tid & 7) == 0) st_lds[tid >> 3] = f32x2{M, R};
      }
    }
  f32x4 sx[2] = {f32x4{0.f, 0.f, 0.f, 0.f}, f32x4{0.f, 0.f, 0.f, 0.f}}, qx[2] = {sx[0], sx[0]};
  static_for<0, C::NB>([&](auto b_) {
    constexpr int s = b_.value;
    if (wid == 0) DBG(MODE, 2, s);
    lds_bar();
    if (wid == 0) DBG(MODE, 0, s);
    if constexpr (s == 0) STAMP(MODE, 1);
    constexpr int S0 = s * C::SPB;
    constexpr int NSUB = ((S0 + C::SPB > C::NSTAGE) ? C::NSTAGE : S0 + C::SPB) - S0;
    constexpr int NKS = 2 * NSUB - ((MODE == 7 && S0 + NSUB == C::NSTAGE) ? 1 : 0);
    bf16x8 xf[NKS][2], wf[NKS][NT];
#pragma unroll
    for (int ks = 0; ks < NKS; ++ks) {
      const lds_cptr bs = (lds_cptr)lds + ((S0 + (ks >> 1)) % C::NSLOT) * C::SLOT;
      const int kk = ks & 1;
#pragma unroll
      for (int nt = 0; nt < NT; ++nt) {
        s16x4 w0 = lds_tr(bs + woff[kk][0] + nt * 128);
        s16x4 w1 = lds_tr(bs + woff[kk][1] + nt * 128);
        wf[ks][nt] = __builtin_shufflevector(w0, w1, 0, 1, 2, 3, 4, 5, 6, 7);
        if (nt == 0) {
#pragma unroll
          for (int mt = 0; mt < 2; ++mt) xf[ks][mt] = lds_rd128(bs + xoff[mt][kk]);
        }
      }
    }
#pragma unroll
    for (int ks = 0; ks < NKS; ++ks) {
#pragma unroll
      for (int nt = 0; nt < NT; ++nt) {
#pragma unroll
        for (int mt = 0; mt < 2; ++mt) {
          if (MODE == 1 && nt == 2) acc[mt][nt] = MFMA16(xf[ks][mt], wf[ks][nt], acc[mt][nt]);
          else acc[mt][nt] = MFMA16(wf[ks][nt], xf[ks][mt], acc[mt][nt]);
        }
      }
    }
    if constexpr (C::LN) {
      auto colsum = [&](auto par_) {
#pragma unroll
        for (int ks = par_.value; ks < NKS; ks += 2)
#pragma unroll
          for (int nt = 0; nt < NT; ++nt) {
            if (MODE == 1 && nt == 2) cs[nt] = MFMA16(ones8, wf[ks][nt], cs[nt]);
            else cs[nt] = MFMA16(wf[ks][nt], ones8, cs[nt]);
          }
      };
      if (wm == 0) colsum(std::integral_constant<int, 0>{});
      else colsum(std::integral_constant<int, 1>{});
    }
    if constexpr (MODE == 1) {
      auto stat = [&](const bf16x8 (&xx)[2]) {
#pragma unroll
        for (int mt = 0; mt < 2; ++mt) {
          sx[mt] = MFMA16(ones8, xx[mt], sx[mt]);
          qx[mt] = MFMA16(xx[mt], xx[mt], qx[mt]);
        }
      };
      if (wn == ((2 * s) & 3)) stat(xf[0]);
      else if (wn == ((2 * s + 1) & 3)) stat(xf[1]);
    }
    __builtin_amdgcn_sched_barrier(0);
  });

  float* cs_lds = (float*)(lds + C::X_CS);
  if constexpr (C::LN) {
#pragma unroll
    for (int nt = 0; nt < NT; ++nt) {
      float* d = cs_lds + ((wm * 4 + wn) * NT + nt) * 16;
      if (MODE == 1 && nt == 2) { if (g == 0) d[l15] = cs[nt][0]; }
      else if (l15 == 0) *(f32x4*)(d + 4 * g) = cs[nt];
    }
    if constexpr (MODE == 1) {
      f32x2* px = (f32x2*)(lds + C::X_PX);
      const int di = l15 & 3;
      float b0 = di == 0 ? qx[0][0] : di == 1 ? qx[0][1] : di == 2 ? qx[0][2] : qx[0][3];
      float b1 = di == 0 ? qx[1][0] : di == 1 ? qx[1][1] : di == 2 ? qx[1][2] : qx[1][3];
      if (g == (l15 >> 2)) { px[wn * 64 + wm * 32 + l15] = f32x2{sx[0][0], b0}; px[wn * 64 + wm * 32 + 16 + l15] = f32x2{sx[1][0], b1}; }
      lds_bar();
      if (tid < 64) {
        f32x2 t = (px[tid] + px[64 + tid]) + (px[128 + tid] + px[192 + tid]);
        float mb, sd, m, v;
        ln_consts(t[0], t[1], mb, sd, m, v);
        st_lds[tid] = f32x2{mb, 1.0f / sd};
      }
    }
    lds_bar();
#pragma unroll
    for (int nt = 0; nt < NT; ++nt) {
      const float* d = cs_lds + (((1 - wm) * 4 + wn) * NT + nt) * 16;
      if (MODE == 1 && nt == 2) cs[nt][0] += d[l15];
      else cs[nt] += *(const f32x4*)(d + 4 * g);
    }
  }
  STAMP(MODE, 2);
  char* ost = lds + C::X_OUT;
#pragma unroll
  for (int mt = 0; mt < 2; ++mt) {
    const int lrow = wm * 32 + mt * 16 + l15;
    const int tok = row0 + lrow;
#pragma unroll
    for (int nt = 0; nt < NT; ++nt) {
      f32x4 a = acc[mt][nt];
      if constexpr (C::LN) {
        if (MODE == 1 && nt == 2) {
          float c = cs[nt][0];
#pragma unroll
          for (int i = 0; i < 4; ++i) {
            f32x2 st = st_lds[wm * 32 + mt * 16 + 4 * g + i];
            a[i] = (a[i] - st[0] * c) * st[1];
          }
        } else {
          f32x4 c = cs[nt];
          f32x2 st = st_lds[lrow];
#pragma unroll
          for (int i = 0; i < 4; ++i) a[i] = (a[i] - st[0] * c[i]) * st[1];
        }
      }
      u32x2 v;
      if constexpr (MODE == 1) {
        const int head = cb * 4 + wn;
        if (nt < 2) {
          float sc = (nt == 0) ? 0.25f * 1.4426950408889634f : 1.0f;
          v = u32x2{cvtpk(a[0] * sc, a[1] * sc), cvtpk(a[2] * sc, a[3] * sc)};
          u16* dst = (nt == 0 ? ga.o0 : ga.o1) + ((size_t)(head * 64 + (tok >> 5)) * 2 + (g >> 1)) * 256 + (tok & 31) * 8 + (g & 1) * 4;
          *(u32x2*)dst = v;
        } else {
          const int step = (row0 + wm * 32 + mt * 16) >> 4;
          v = u32x2{cvtpk(a[0], a[1]), cvtpk(a[2], a[3])};
          u16* dst = ga.o2 + ((size_t)(head * 128 + step) * 2 + (g & 1)) * 128 + l15 * 8 + (g >> 1) * 4;
          *(u32x2*)dst = v;
        }
      } else if constexpr (MODE == 3 || MODE == 5) {
        u32x2 r = res0[mt];
        if constexpr (MODE == 5) {
          f32x2 st = st_lds[lrow];
          unsigned d0 = cvtpk(lo2f(r[0]) - st[0], hi2f(r[0]) - st[0]), d1 = cvtpk(lo2f(r[1]) - st[0], hi2f(r[1]) - st[0]);
          r[0] = cvtpk(lo2f(d0) * st[1], hi2f(d0) * st[1]);
          r[1] = cvtpk(lo2f(d1) * st[1], hi2f(d1) * st[1]);
        }
        unsigned a0 = cvtpk(a[0], a[1]), a1 = cvtpk(a[2], a[3]);
        v = u32x2{cvtpk(lo2f(r[0]) + lo2f(a0), hi2f(r[0]) + hi2f(a0)), cvtpk(lo2f(r[1]) + lo2f(a1), hi2f(r[1]) + hi2f(a1))};
        float f0 = lo2f(v[0]), f1 = hi2f(v[0]), f2 = lo2f(v[1]), f3 = hi2f(v[1]);
        float s_ = (f0 + f1) + (f2 + f3), q_ = fmaf(f0, f0, f1 * f1) + fmaf(f2, f2, f3 * f3);
        s_ = add_x32(add_x16(s_)); q_ = add_x32(add_x16(q_));
        if (g == 0) *(f32x2*)(ga.pout + (size_t)tok * 64 + (cb * 4 + wn) * 2) = f32x2{s_, q_};
      } else if constexpr (MODE == 4 || MODE == 6) {
        unsigned a0 = cvtpk(a[0], a[1]), a1 = cvtpk(a[2], a[3]);
        v = u32x2{cvtpk(gelu_tanh(lo2f(a0)), gelu_tanh(hi2f(a0))), cvtpk(gelu_tanh(lo2f(a1)), gelu_tanh(hi2f(a1)))};
      } else {
        u32x2 x1 = res0[mt], hh = res1[mt];
        unsigned a0 = cvtpk(a[0], a[1]), a1 = cvtpk(a[2], a[3]);
        unsigned b0 = cvtpk(lo2f(x1[0]) + lo2f(a0), hi2f(x1[0]) + hi2f(a0));
        unsigned b1 = cvtpk(lo2f(x1[1]) + lo2f(a1), hi2f(x1[1]) + hi2f(a1));
        v = u32x2{cvtpk(lo2f(hh[0]) + lo2f(b0), hi2f(hh[0]) + hi2f(b0)), cvtpk(lo2f(hh[1]) + lo2f(b1), hi2f(hh[1]) + hi2f(b1))};
      }
      if constexpr (MODE != 1) *(u32x2*)(ost + lrow * C::RSO + nt * 128 + wn * 32 + g * 8) = v;
    }
  }
  if constexpr (MODE != 1) {
    lds_bar();
#pragma unroll
    for (int j = 0; j < NT; ++j) {
      const int c = tid + 512 * j, row = c / (8 * NT), cc = c % (8 * NT);
      const u32x4 v = *(const u32x4*)(ost + row * C::RSO + cc * 16);
      const int tok = row0 + row;
      if constexpr (MODE == 4) {
        const int grp = cb >> 1;
        const int sh = (grp == 0) ? 1 : (grp == 1) ? -1 : (grp == 2) ? 2 : -2;
        const int col = cb * 64 + cc * 8, dt = tok + sh, src = tok - sh;
        if (dt >= 0 && dt < SEQ) *(u32x4*)(ga.o0 + (size_t)dt * DM + col) = v;
        if (src < 0 || src >= SEQ) *(u32x4*)(ga.o0 + (size_t)tok * DM + col) = u32x4{0u, 0u, 0u, 0u};
      } else if constexpr (MODE == 6) {
        const int col = cb * 192 + cc * 8;
        if (col < FF) *(u32x4*)(ga.o0 + (size_t)tok * FF + col) = v;
      } else {
        *(u32x4*)(ga.o0 + (size_t)tok * DM + cb * 64 + cc * 8) = v;
      }
    }
  }
  STAMP(MODE, 3);
}

constexpr int AT_K = 0, AT_V = 65536, AT_ONES = 131072, AT_Q = AT_ONES + 8192, AT_LDS = AT_Q + 8192;

__global__ __launch_bounds__(1024, 4) void attn_kernel(const u16* Qf, const u16* Kf, const u16* Vf, u16* O, const u16* wa,
                                                       const u16* wb, const u16* wc, const u16* wd, const u16* we) {
  extern __shared__ __attribute__((aligned(16))) char lds[];
  STAMP(2, 0);
  const int tid = threadIdx.x, lane = tid & 63;
  const int wid = __builtin_amdgcn_readfirstlane(tid >> 6);
  const int xcd = blockIdx.x & 7, idx = blockIdx.x >> 3;
  const int head = xcd * 4 + (idx >> 3), qb = idx & 7;
  const unsigned lds0 = (unsigned)(uintptr_t)lds;
  const int r = lane & 31, h = lane >> 5;
  const char* kg = (const char*)Kf + (size_t)head * 65536;
  const char* vg = (const char*)Vf + (size_t)head * 65536;
  const int qt = wid & 7, kh = wid >> 3;
  const char* qg = (const char*)Qf + (size_t)head * 65536 + (qb * 8 + qt) * 1024;
  const unsigned lo = lane * 16, pc = (unsigned)(kh * 32768 + qt * 1024);

  if (kh == 0) glds16(qg, lo, lds0 + AT_Q + qt * 1024);
#pragma unroll
  for (int c = 0; c < 2; ++c) {
    glds16(kg, lo + c * 8192 + pc, lds0 + AT_K + c * 8192 + pc);
    glds16(vg, lo + c * 8192 + pc, lds0 + AT_V + c * 8192 + pc);
  }
  *(u32x2*)(lds + AT_ONES + tid * 8) = u32x2{0x3F803F80u, 0x3F803F80u};

  lds_cptr kp = (lds_cptr)lds + AT_K + kh * 32768 + lane * 16;
  lds_cptr vp = (lds_cptr)lds + ((r < 16) ? (AT_V + kh * 32768 + h * 256 + r * 16) : (AT_ONES + (r - 16) * 16));
  const int vstep = (r < 16) ? 4096 : 0;

  f32x16 o, negm, sA, sB;
#pragma unroll
  for (int i = 0; i < 16; ++i) { o[i] = 0.f; negm[i] = 0.f; }

  wait_vm_bar<0>();
  STAMP(2, 1);
#pragma unroll
  for (int c = 2; c < 4; ++c) {
    glds16(kg, lo + c * 8192 + pc, lds0 + AT_K + c * 8192 + pc);
    glds16(vg, lo + c * 8192 + pc, lds0 + AT_V + c * 8192 + pc);
  }
  unsigned tw[5];
  {
    unsigned t = (unsigned)(idx * 1024 + tid) * 128u;
    unsigned ta = t < 512u * 1024u ? t : 0u, tb = t < (unsigned)(DM * FF * 2) ? t : 0u;
    asm volatile("global_load_dword %0, %5, %7\n\tglobal_load_dword %1, %5, %8\n\tglobal_load_dword %2, %5, %9\n\t"
                 "global_load_dword %3, %6, %10\n\tglobal_load_dword %4, %6, %11"
                 : "=&v"(tw[0]), "=&v"(tw[1]), "=&v"(tw[2]), "=&v"(tw[3]), "=&v"(tw[4])
                 : "v"(ta), "v"(tb), "s"(wa), "s"(wb), "s"(wc), "s"(wd), "s"(we)
                 : "memory");
  }
  const bf16x8 qf = lds_rd128((lds_cptr)lds + AT_Q + qt * 1024 + lane * 16);
  bf16x8 kfA, kfB, vA0, vA1, vB0, vB1;
  u32x4 pA0, pA1, pB0, pB1;
  {
    sA = MFMA32(lds_rd128(kp), qf, negm);
    kfA = lds_rd128(kp + 1024);
    float mx = sA[0];
#pragma unroll
    for (int i = 1; i < 16; ++i) mx = fmaxf(mx, sA[i]);
    const float d = max_x32(mx) + 7.0f;
#pragma unroll
    for (int i = 0; i < 16; ++i) { negm[i] = -d; sA[i] -= d; }
    vB0 = lds_rd128(vp); vB1 = vB0;
    pB0 = u32x4{0u, 0u, 0u, 0u}; pB1 = pB0;
  }

#define SBAR() __builtin_amdgcn_sched_barrier(0)
#define PK(x, i) __builtin_amdgcn_perm(__float_as_uint(x[(i) + 1]), __float_as_uint(x[i]), 0x07060302u)
#define EXPS(lo_, hi_) _Pragma("unroll") for (int i = lo_; i < hi_; ++i) sc[i] = __builtin_amdgcn_exp2f(sc[i])
  auto step = [&](f32x16& sc, f32x16& sn, const bf16x8& kf, bf16x8& kfn, const bf16x8& vp0, const bf16x8& vp1,
                  const u32x4& pp0, const u32x4& pp1, bf16x8& vc0, bf16x8& vc1, u32x4& pc0, u32x4& pc1, lds_cptr kc,
                  lds_cptr vc) {
    sn = MFMA32(kf, qf, negm);
    kfn = lds_rd128(kc + 2048);
    vc0 = lds_rd128(vc); vc1 = lds_rd128(vc + 512);
    SBAR();
    EXPS(0, 5);
    SBAR();
    o = MFMA32(vp0, __builtin_bit_cast(bf16x8, pp0), o);
    SBAR();
    EXPS(5, 11);
    SBAR();
    o = MFMA32(vp1, __builtin_bit_cast(bf16x8, pp1), o);
    SBAR();
    EXPS(11, 16);
    SBAR();
#pragma unroll
    for (int i = 0; i < 4; ++i) { pc0[i] = PK(sc, 2 * i); pc1[i] = PK(sc, 8 + 2 * i); }
    unsigned chk = (pc0[0] | pc0[1] | pc0[2]) | (pc0[3] | pc1[0] | pc1[1]) | (pc1[2] | pc1[3]);
    if (__builtin_expect(__any((chk & 0x40004000u) != 0), 0)) {
      f32x16 s2 = MFMA32(lds_rd128(kc), qf, negm);
      float mx = s2[0];
#pragma unroll
      for (int i = 1; i < 16; ++i) mx = fmaxf(mx, s2[i]);
      const float d = fmaxf(max_x32(mx) + 7.0f, 0.f);
      const float alpha = __builtin_amdgcn_exp2f(-d);
#pragma unroll
      for (int i = 0; i < 16; ++i) { o[i] *= alpha; negm[i] -= d; sn[i] -= d; s2[i] = __builtin_amdgcn_exp2f(s2[i] - d); }
#pragma unroll
      for (int i = 0; i < 4; ++i) { pc0[i] = PK(s2, 2 * i); pc1[i] = PK(s2, 8 + 2 * i); }
    }
    SBAR();
  };
  auto group = [&]() {
    step(sA, sB, kfA, kfB, vB0, vB1, pB0, pB1, vA0, vA1, pA0, pA1, kp, vp);
    step(sB, sA, kfB, kfA, vA0, vA1, pA0, pA1, vB0, vB1, pB0, pB1, kp + 1024, vp + 1024);
    step(sA, sB, kfA, kfB, vB0, vB1, pB0, pB1, vA0, vA1, pA0, pA1, kp + 2048, vp + 2048);
    step(sB, sA, kfB, kfA, vA0, vA1, pA0, pA1, vB0, vB1, pB0, pB1, kp + 3072, vp + 3072);
    kp += 4096;
    vp += vstep;
  };
#pragma unroll 1
  for (int it = 0; it < 3; ++it) group();
  asm volatile("s_waitcnt vmcnt(0) lgkmcnt(0)\n\ts_barrier" : "+v"(tw[0]), "+v"(tw[1]), "+v"(tw[2]), "+v"(tw[3]), "+v"(tw[4])::"memory");
#pragma unroll 1
  for (int it = 3; it < 8; ++it) group();
  o = MFMA32(vB0, __builtin_bit_cast(bf16x8, pB0), o);
  o = MFMA32(vB1, __builtin_bit_cast(bf16x8, pB1), o);

  STAMP(2, 2);
  float* mg = (float*)(lds + AT_K) + qt * 640 + lane;
  lds_bar();
  if (kh == 1) {
#pragma unroll
    for (int i = 0; i < 9; ++i) mg[i * 64] = o[i];
    mg[9 * 64] = negm[0];
  }
  lds_bar();
  if (kh == 0) {
    const float n1 = mg[9 * 64], n0 = negm[0];
    const float nm = fminf(n0, n1);
    const float a0 = __builtin_amdgcn_exp2f(nm - n0), a1 = __builtin_amdgcn_exp2f(nm - n1);
#pragma unroll
    for (int i = 0; i < 9; ++i) o[i] = o[i] * a0 + mg[i * 64] * a1;
    const float inv = 1.0f / o[8];
    const int q = qb * 256 + qt * 32 + r;
    u16* dst = O + (size_t)q * DM + head * 16 + 4 * h;
    *(u32x2*)dst = u32x2{cvtpk(o[0] * inv, o[1] * inv), cvtpk(o[2] * inv, o[3] * inv)};
    *(u32x2*)(dst + 8) = u32x2{cvtpk(o[4] * inv, o[5] * inv), cvtpk(o[6] * inv, o[7] * inv)};
  }
  STAMP(2, 3);
}

template <int MODE>
static void launch_gemm(const GArgs& ga, hipStream_t stream) {
  (void)hipFuncSetAttribute((const void*)gemm_kernel<MODE>, hipFuncAttributeMaxDynamicSharedMemorySize, Cfg<MODE>::LDS);
  gemm_kernel<MODE><<<256, 512 + 64 * NLD, Cfg<MODE>::LDS, stream>>>(ga);
}

extern "C" void kernel_launch(void* const* d_in, const int* in_sizes, int n_in, void* d_out, int out_size, void* d_ws,
                              size_t ws_size, hipStream_t stream) {
  const u16* x = (const u16*)d_in[0];
  const u16* wqkv = (const u16*)d_in[1];
  const u16* wo = (const u16*)d_in[2];
  const u16* w1 = (const u16*)d_in[4];
  const u16* w2 = (const u16*)d_in[5];
  const u16* w3 = (const u16*)d_in[6];
  const u16* w4 = (const u16*)d_in[7];
  char* ws = (char*)d_ws;
  u16 *Q = (u16*)(ws + WS_Q), *Kf = (u16*)(ws + WS_K), *Vf = (u16*)(ws + WS_V), *O = (u16*)(ws + WS_O),
      *H = (u16*)(ws + WS_H), *T = (u16*)(ws + WS_T), *X1 = (u16*)(ws + WS_X1), *U = (u16*)(ws + WS_U);
  float *PH = (float*)(ws + WS_PH), *PX = (float*)(ws + WS_PX);
  u16* out = (u16*)d_out;

  launch_gemm<1>(GArgs{x, wqkv, nullptr, nullptr, nullptr, nullptr, Q, Kf, Vf}, stream);
  (void)hipFuncSetAttribute((const void*)attn_kernel, hipFuncAttributeMaxDynamicSharedMemorySize, AT_LDS);
  attn_kernel<<<256, 1024, AT_LDS, stream>>>(Q, Kf, Vf, O, wo, w1, w2, w3, w4);
  launch_gemm<3>(GArgs{O, wo, x, nullptr, nullptr, PH, H, nullptr, nullptr}, stream);
  launch_gemm<4>(GArgs{H, w1, nullptr, nullptr, PH, nullptr, T, nullptr, nullptr}, stream);
  launch_gemm<5>(GArgs{T, w2, H, nullptr, PH, PX, X1, nullptr, nullptr}, stream);
  launch_gemm<6>(GArgs{X1, w3, nullptr, nullptr, PX, nullptr, U, nullptr, nullptr}, stream);
  launch_gemm<7>(GArgs{U, w4, X1, H, nullptr, nullptr, out, nullptr, nullptr}, stream);
}
```

```cpp
#include <hip/hip_runtime.h>
#include <hip/hip_bf16.h>
#include <stdint.h>

typedef unsigned short u16;
typedef __attribute__((ext_vector_type(8))) short bf16x8;
typedef __attribute__((ext_vector_type(4))) short s16x4;
typedef __attribute__((ext_vector_type(2))) float f32x2;
typedef __attribute__((ext_vector_type(4))) float f32x4;
typedef __attribute__((ext_vector_type(16))) float f32x16;
typedef __attribute__((ext_vector_type(2))) __bf16 bf16x2_t;
typedef __attribute__((ext_vector_type(4))) unsigned u32x4;
typedef __attribute__((ext_vector_type(2))) unsigned u32x2;
typedef short v4i16_t __attribute__((ext_vector_type(4)));
typedef __attribute__((address_space(3))) const char* lds_cptr;

#define DEV __device__ __forceinline__
#define MFMA16(a, b, c) __builtin_amdgcn_mfma_f32_16x16x32_bf16((a), (b), (c), 0, 0, 0)
#define MFMA32(a, b, c) __builtin_amdgcn_mfma_f32_32x32x16_bf16((a), (b), (c), 0, 0, 0)

constexpr int SEQ = 2048, DM = 512, NH = 32, FF = 1376;

constexpr size_t WS_Q = 0, WS_K = 2u << 20, WS_V = 4u << 20, WS_O = 6u << 20, WS_H = 8u << 20, WS_PH = 10u << 20,
                 WS_PX = 11u << 20, WS_T = 12u << 20, WS_X1 = 14u << 20, WS_U = 16u << 20;

DEV unsigned cvtpk(float lo, float hi) {
  f32x2 v = {lo, hi};
  bf16x2_t b = __builtin_convertvector(v, bf16x2_t);
  return __builtin_bit_cast(unsigned, b);
}
DEV float lo2f(unsigned p) { return __uint_as_float(p << 16); }
DEV float hi2f(unsigned p) { return __uint_as_float(p & 0xffff0000u); }
DEV float rbf(float x) { return lo2f(cvtpk(x, x)); }

DEV void glds16(const void* gbase, unsigned voff, unsigned lds_base) {
  unsigned sv;
  asm volatile("s_mov_b32 %0, m0\n\ts_mov_b32 m0, %3\n\ts_nop 0\n\tglobal_load_lds_dwordx4 %1, %2\n\ts_mov_b32 m0, %0"
               : "=&s"(sv)
               : "v"(voff), "s"(gbase), "s"(lds_base)
               : "memory");
}
DEV u32x4 aload4(const void* gbase, unsigned voff) {
  u32x4 r;
  asm volatile("global_load_dwordx4 %0, %1, %2" : "=v"(r) : "v"(voff), "s"(gbase) : "memory");
  return r;
}
DEV u32x2 aload2(const void* gbase, unsigned voff) {
  u32x2 r;
  asm volatile("global_load_dwordx2 %0, %1, %2" : "=v"(r) : "v"(voff), "s"(gbase) : "memory");
  return r;
}
DEV void st_wt4(void* p, u32x4 v) { asm volatile("global_store_dwordx4 %0, %1, off sc1\n\ts_nop 1" ::"v"(p), "v"(v) : "memory"); }
DEV void st_wt2(void* p, u32x2 v) { asm volatile("global_store_dwordx2 %0, %1, off sc1\n\ts_nop 1" ::"v"(p), "v"(v) : "memory"); }
template <class T>
DEV void pin(T& x) { asm volatile("" : "+v"(x)); }
template <int N>
DEV void wait_vm_bar() {
  asm volatile("s_waitcnt vmcnt(%0) lgkmcnt(0)\n\ts_barrier" ::"i"(N) : "memory");
}
DEV void lds_bar() { asm volatile("s_waitcnt lgkmcnt(0)\n\ts_barrier" ::: "memory"); }
DEV bf16x8 lds_rd128(lds_cptr p) { return *(const __attribute__((address_space(3))) bf16x8*)p; }
DEV s16x4 lds_tr(lds_cptr p) {
  return __builtin_bit_cast(s16x4, __builtin_amdgcn_ds_read_tr16_b64_v4i16((__attribute__((address_space(3))) v4i16_t*)p));
}
template <int CTRL>
DEV float dpp_add(float v) {
  int x = __builtin_amdgcn_update_dpp(0, __float_as_int(v), CTRL, 0xf, 0xf, true);
  return v + __int_as_float(x);
}
DEV float add_x16(float v) {
  auto p = __builtin_amdgcn_permlane16_swap(__float_as_uint(v), __float_as_uint(v), false, false);
  return __uint_as_float(p[0]) + __uint_as_float(p[1]);
}
DEV float add_x32(float v) {
  auto p = __builtin_amdgcn_permlane32_swap(__float_as_uint(v), __float_as_uint(v), false, false);
  return __uint_as_float(p[0]) + __uint_as_float(p[1]);
}
DEV float max_x32(float v) {
  auto p = __builtin_amdgcn_permlane32_swap(__float_as_uint(v), __float_as_uint(v), false, false);
  return fmaxf(__uint_as_float(p[0]), __uint_as_float(p[1]));
}
DEV float wave_sum(float v) {
  v = dpp_add<0x121>(v);
  v = dpp_add<0x122>(v);
  v = dpp_add<0x124>(v);
  v = dpp_add<0x128>(v);
  return add_x32(add_x16(v));
}
DEV float dot2_ones(unsigned p, float acc) {
  return __builtin_amdgcn_fdot2_f32_bf16(__builtin_bit_cast(bf16x2_t, p), __builtin_bit_cast(bf16x2_t, 0x3F803F80u), acc, false);
}
DEV float dot2u(unsigned a, unsigned b, float acc) {
  return __builtin_amdgcn_fdot2_f32_bf16(__builtin_bit_cast(bf16x2_t, a), __builtin_bit_cast(bf16x2_t, b), acc, false);
}
DEV float gelu_tanh(float x) {
  const float c = -2.0f * 0.7978845608028654f * 1.4426950408889634f;
  float u = x * (1.0f + 0.044715f * x * x);
  float e = __builtin_amdgcn_exp2f(c * u);
  return x * __builtin_amdgcn_rcpf(1.0f + e);
}
DEV int bswz(int k) { return ((k >> 1) & 1) | (((k >> 3) & 1) << 1); }

#define STAMP(kid, i) do { } while (0)
#define DBG(kid, w, i) do { } while (0)

template <int I, int N, class F>
DEV void static_for(F&& f) {
  if constexpr (I < N) {
    f(std::integral_constant<int, I>{});
    static_for<I + 1, N>(f);
  }
}

DEV void ln_consts(float s, float q, float& mb, float& sd, float& m, float& v) {
  m = s * (1.0f / DM);
  v = fmaxf(q * (1.0f / DM) - m * m, 0.f);
  mb = rbf(m);
  sd = rbf(__builtin_sqrtf(rbf(rbf(v) + 1e-5f)));
}

constexpr int NLD = 4;
template <int MODE>
struct Cfg {
  static constexpr int NT = (MODE == 1 || MODE == 6) ? 3 : 1;
  static constexpr bool LN = (MODE == 1 || MODE == 4 || MODE == 6);
  static constexpr bool PST = (MODE == 4 || MODE == 5 || MODE == 6);
  static constexpr int K = (MODE == 7) ? FF : DM;
  static constexpr int LDA = K;
  static constexpr int LDW = (MODE == 1) ? 3 * DM : (MODE == 6 ? FF : DM);
  static constexpr int NSTAGE = (K + 63) / 64;
  static constexpr int SPB = (NT == 1) ? 2 : 1;
  static constexpr int NB = (NSTAGE + SPB - 1) / SPB;
  static constexpr int RS = 128 * NT;
  static constexpr int BBYTES = 64 * RS;
  static constexpr int SLOT = BBYTES + 8192;
  static constexpr int NGSLOT = 4;
  static constexpr int NSLOT = NGSLOT * SPB;
  static constexpr int DEPTH = NGSLOT - 1;
  static constexpr int X_ST = NSLOT * SLOT;
  static constexpr int X_CS = X_ST + 512;
  static constexpr int X_PX = X_CS + 1536;
  static constexpr int X_OUT = X_PX + 2048;
  static constexpr int RSO = 128 * NT + 16;
  static constexpr int LDS = X_OUT + (MODE == 1 ? 0 : 64 * RSO);
  static constexpr int LPS = 8 * (NT + 1) / NLD;
};

struct GArgs {
  const u16* A;
  const u16* W;
  const u16* r0;
  const u16* r1;
  const float* pin_;
  float* pout;
  u16* o0;
  u16* o1;
  u16* o2;
};

template <int MODE>
__global__ __launch_bounds__(512 + 64 * NLD, 2) void gemm_kernel(GArgs ga) {
  using C = Cfg<MODE>;
  constexpr int NT = C::NT;
  extern __shared__ __attribute__((aligned(16))) char lds[];
  STAMP(MODE, 0);
  const int tid = threadIdx.x, lane = tid & 63;
  const int wid = __builtin_amdgcn_readfirstlane(tid >> 6);
  const int xcd = blockIdx.x & 7, idx = blockIdx.x >> 3;
  const int rb = xcd * 4 + (idx >> 3), cb = idx & 7;
  const int row0 = rb * 64;
  const unsigned lds0 = (unsigned)(uintptr_t)lds;

  if (wid >= 8) {
    const int L = wid - 8;
    constexpr int IA = 8 / NLD, IB = 8 * NT / NLD;
    unsigned boff[IB];
    int bk[IB];
#pragma unroll
    for (int j = 0; j < IB; ++j) {
      int o = (L * IB + j) * 1024 + lane * 16;
      int k = o / C::RS, rem = o % C::RS;
      int grp = rem >> 7, blk = (rem >> 5) & 3, hf = (rem >> 4) & 1;
      int col;
      if (MODE == 1) col = grp * 512 + cb * 64;
      else col = cb * (64 * NT) + grp * 64;
      col += ((blk ^ bswz(k)) * 16) + hf * 8;
      if (MODE == 6 && col > FF - 8) col = FF - 8;
      bk[j] = k;
      boff[j] = (unsigned)(k * C::LDW + col) * 2u;
    }
    unsigned aoffj[IA];
#pragma unroll
    for (int j = 0; j < IA; ++j) {
      int r = (L * IA + j) * 8 + (lane >> 3), c = (lane & 7) ^ ((r >> 1) & 7);
      aoffj[j] = (unsigned)((row0 + r) * C::LDA + c * 8) * 2u;
    }
    auto issue_sub = [&](int s) {
      const unsigned slot = lds0 + (unsigned)((s % C::NSLOT) * C::SLOT);
#pragma unroll
      for (int j = 0; j < IA; ++j) glds16(ga.A, aoffj[j] + s * 128, slot + C::BBYTES + (L * IA + j) * 1024);
#pragma unroll
      for (int j = 0; j < IB; ++j) {
        unsigned off = boff[j] + (unsigned)(s * 64 * C::LDW) * 2u;
        if (MODE == 7 && s == C::NSTAGE - 1 && s * 64 + bk[j] >= FF) off = boff[j] + (unsigned)((FF - 64) * C::LDW) * 2u;
        glds16(ga.W, off, slot + (unsigned)(L * IB + j) * 1024u);
      }
    };
    auto issue_group = [&](auto g_) {
      constexpr int gg = g_.value;
      static_for<0, C::SPB>([&](auto u_) { if constexpr (gg < C::NB && gg * C::SPB + u_.value < C::NSTAGE) issue_sub(gg * C::SPB + u_.value); });
    };
    issue_group(std::integral_constant<int, 0>{});
    issue_group(std::integral_constant<int, 1>{});
    static_for<0, C::NB>([&](auto b_) {
      constexpr int b = b_.value;
      constexpr int lastg0 = (b == 0) ? 1 : b + C::DEPTH - 1;
      constexpr int lastg = lastg0 < C::NB - 1 ? lastg0 : C::NB - 1;
      constexpr int sub_after = ((lastg + 1) * C::SPB > C::NSTAGE ? C::NSTAGE : (lastg + 1) * C::SPB) - ((b + 1) * C::SPB > C::NSTAGE ? C::NSTAGE : (b + 1) * C::SPB);
      asm volatile("s_waitcnt vmcnt(%0)" ::"i"(sub_after * C::LPS) : "memory");
      if (wid == 8) DBG(MODE, 1, b);
      wait_vm_bar<sub_after * C::LPS>();
      if constexpr (b == 0) {
        static_for<2, C::DEPTH + 1>([&](auto g_) { issue_group(g_); });
      } else {
        issue_group(std::integral_constant<int, b + C::DEPTH>{});
      }
    });
    return;
  }

  const int wm = wid & 1, wn = wid >> 1;
  const int l15 = lane & 15, g = lane >> 4;
  u32x4 ps[2];
  u32x2 res0[2], res1[2];
  if constexpr (C::PST) {
    const float* pp = ga.pin_ + (size_t)(row0 + (tid >> 3)) * 64 + (tid & 7) * 8;
    ps[0] = *(const u32x4*)pp;
    ps[1] = *(const u32x4*)(pp + 4);
  }
  if constexpr (MODE == 3 || MODE == 5 || MODE == 7) {
#pragma unroll
    for (int mt = 0; mt < 2; ++mt) {
      size_t o = (size_t)(row0 + wm * 32 + mt * 16 + l15) * DM + cb * 64 + wn * 16 + 4 * g;
      res0[mt] = *(const u32x2*)(ga.r0 + o);
      if constexpr (MODE == 7) res1[mt] = *(const u32x2*)(ga.r1 + o);
    }
  }

  int xoff[2][2];
#pragma unroll
  for (int mt = 0; mt < 2; ++mt)
#pragma unroll
    for (int kk = 0; kk < 2; ++kk) {
      int r = wm * 32 + mt * 16 + l15;
      xoff[mt][kk] = C::BBYTES + r * 128 + (((4 * kk + g) ^ ((r >> 1) & 7)) * 16);
    }
  int woff[2][2];
  {
    int q = l15 >> 2, p = l15 & 3;
#pragma unroll
    for (int kk = 0; kk < 2; ++kk)
#pragma unroll
      for (int t = 0; t < 2; ++t) {
        int k = 32 * kk + 8 * g + 4 * t + q;
        woff[kk][t] = k * C::RS + ((wn ^ bswz(k)) * 32) + p * 8;
      }
  }

  f32x4 acc[2][NT];
  f32x4 cs[NT];
  const bf16x8 ones8 = {0x3F80, 0x3F80, 0x3F80, 0x3F80, 0x3F80, 0x3F80, 0x3F80, 0x3F80};
#pragma unroll
  for (int nt = 0; nt < NT; ++nt) {
    cs[nt] = f32x4{0.f, 0.f, 0.f, 0.f};
#pragma unroll
    for (int mt = 0; mt < 2; ++mt) acc[mt][nt] = f32x4{0.f, 0.f, 0.f, 0.f};
  }
  f32x2* st_lds = (f32x2*)(lds + C::X_ST);

    {
      if constexpr (C::PST) {
        float s_ = (__uint_as_float(ps[0][0]) + __uint_as_float(ps[0][2])) + (__uint_as_float(ps[1][0]) + __uint_as_float(ps[1][2]));
        float q_ = (__uint_as_float(ps[0][1]) + __uint_as_float(ps[0][3])) + (__uint_as_float(ps[1][1]) + __uint_as_float(ps[1][3]));
        s_ = dpp_add<0xB1>(s_); q_ = dpp_add<0xB1>(q_);
        s_ = dpp_add<0x4E>(s_); q_ = dpp_add<0x4E>(q_);
        s_ = dpp_add<0x141>(s_); q_ = dpp_add<0x141>(q_);
        float mb, sd, m, v;
        ln_consts(s_, q_, mb, sd, m, v);
        float M = mb, R = 1.0f / sd;
        if constexpr (MODE == 4) {
          float m2 = (m - mb) * R, v2 = v * R * R;
          float m2b = rbf(m2);
          float sd2 = rbf(__builtin_sqrtf(rbf(rbf(v2) + 1e-5f)));
          M = mb + m2b * sd;
          R = R / sd2;
        }
        if ((tid & 7) == 0) st_lds[tid >> 3] = f32x2{M, R};
      }
    }
  f32x4 sx[2] = {f32x4{0.f, 0.f, 0.f, 0.f}, f32x4{0.f, 0.f, 0.f, 0.f}}, qx[2] = {sx[0], sx[0]};
  static_for<0, C::NB>([&](auto b_) {
    constexpr int s = b_.value;
    if (wid == 0) DBG(MODE, 2, s);
    lds_bar();
    if (wid == 0) DBG(MODE, 0, s);
    if constexpr (s == 0) STAMP(MODE, 1);
    constexpr int S0 = s * C::SPB;
    constexpr int NSUB = ((S0 + C::SPB > C::NSTAGE) ? C::NSTAGE : S0 + C::SPB) - S0;
    constexpr int NKS = 2 * NSUB - ((MODE == 7 && S0 + NSUB == C::NSTAGE) ? 1 : 0);
    bf16x8 xf[NKS][2], wf[NKS][NT];
#pragma unroll
    for (int ks = 0; ks < NKS; ++ks) {
      const lds_cptr bs = (lds_cptr)lds + ((S0 + (ks >> 1)) % C::NSLOT) * C::SLOT;
      const int kk = ks & 1;
#pragma unroll
      for (int nt = 0; nt < NT; ++nt) {
        s16x4 w0 = lds_tr(bs + woff[kk][0] + nt * 128);
        s16x4 w1 = lds_tr(bs + woff[kk][1] + nt * 128);
        wf[ks][nt] = __builtin_shufflevector(w0, w1, 0, 1, 2, 3, 4, 5, 6, 7);
        if (nt == 0) {
#pragma unroll
          for (int mt = 0; mt < 2; ++mt) xf[ks][mt] = lds_rd128(bs + xoff[mt][kk]);
        }
      }
    }
#pragma unroll
    for (int ks = 0; ks < NKS; ++ks) {
#pragma unroll
      for (int nt = 0; nt < NT; ++nt) {
#pragma unroll
        for (int mt = 0; mt < 2; ++mt) {
          if (MODE == 1 && nt == 2) acc[mt][nt] = MFMA16(xf[ks][mt], wf[ks][nt], acc[mt][nt]);
          else acc[mt][nt] = MFMA16(wf[ks][nt], xf[ks][mt], acc[mt][nt]);
        }
      }
    }
    if constexpr (C::LN) {
      auto colsum = [&](auto par_) {
#pragma unroll
        for (int ks = par_.value; ks < NKS; ks += 2)
#pragma unroll
          for (int nt = 0; nt < NT; ++nt) {
            if (MODE == 1 && nt == 2) cs[nt] = MFMA16(ones8, wf[ks][nt], cs[nt]);
            else cs[nt] = MFMA16(wf[ks][nt], ones8, cs[nt]);
          }
      };
      if (wm == 0) colsum(std::integral_constant<int, 0>{});
      else colsum(std::integral_constant<int, 1>{});
    }
    if constexpr (MODE == 1) {
      auto stat = [&](const bf16x8 (&xx)[2]) {
#pragma unroll
        for (int mt = 0; mt < 2; ++mt) {
          sx[mt] = MFMA16(ones8, xx[mt], sx[mt]);
          qx[mt] = MFMA16(xx[mt], xx[mt], qx[mt]);
        }
      };
      if (wn == ((2 * s) & 3)) stat(xf[0]);
      else if (wn == ((2 * s + 1) & 3)) stat(xf[1]);
    }
    __builtin_amdgcn_sched_barrier(0);
  });

  float* cs_lds = (float*)(lds + C::X_CS);
  if constexpr (C::LN) {
#pragma unroll
    for (int nt = 0; nt < NT; ++nt) {
      float* d = cs_lds + ((wm * 4 + wn) * NT + nt) * 16;
      if (MODE == 1 && nt == 2) { if (g == 0) d[l15] = cs[nt][0]; }
      else if (l15 == 0) *(f32x4*)(d + 4 * g) = cs[nt];
    }
    if constexpr (MODE == 1) {
      f32x2* px = (f32x2*)(lds + C::X_PX);
      const int di = l15 & 3;
      float b0 = di == 0 ? qx[0][0] : di == 1 ? qx[0][1] : di == 2 ? qx[0][2] : qx[0][3];
      float b1 = di == 0 ? qx[1][0] : di == 1 ? qx[1][1] : di == 2 ? qx[1][2] : qx[1][3];
      if (g == (l15 >> 2)) { px[wn * 64 + wm * 32 + l15] = f32x2{sx[0][0], b0}; px[wn * 64 + wm * 32 + 16 + l15] = f32x2{sx[1][0], b1}; }
      lds_bar();
      if (tid < 64) {
        f32x2 t = (px[tid] + px[64 + tid]) + (px[128 + tid] + px[192 + tid]);
        float mb, sd, m, v;
        ln_consts(t[0], t[1], mb, sd, m, v);
        st_lds[tid] = f32x2{mb, 1.0f / sd};
      }
    }
    lds_bar();
#pragma unroll
    for (int nt = 0; nt < NT; ++nt) {
      const float* d = cs_lds + (((1 - wm) * 4 + wn) * NT + nt) * 16;
      if (MODE == 1 && nt == 2) cs[nt][0] += d[l15];
      else cs[nt] += *(const f32x4*)(d + 4 * g);
    }
  }
  STAMP(MODE, 2);
  char* ost = lds + C::X_OUT;
#pragma unroll
  for (int mt = 0; mt < 2; ++mt) {
    const int lrow = wm * 32 + mt * 16 + l15;
    const int tok = row0 + lrow;
#pragma unroll
    for (int nt = 0; nt < NT; ++nt) {
      f32x4 a = acc[mt][nt];
      if constexpr (C::LN) {
        if (MODE == 1 && nt == 2) {
          float c = cs[nt][0];
#pragma unroll
          for (int i = 0; i < 4; ++i) {
            f32x2 st = st_lds[wm * 32 + mt * 16 + 4 * g + i];
            a[i] = (a[i] - st[0] * c) * st[1];
          }
        } else {
          f32x4 c = cs[nt];
          f32x2 st = st_lds[lrow];
#pragma unroll
          for (int i = 0; i < 4; ++i) a[i] = (a[i] - st[0] * c[i]) * st[1];
        }
      }
      u32x2 v;
      if constexpr (MODE == 1) {
        const int head = cb * 4 + wn;
        if (nt < 2) {
          float sc = (nt == 0) ? 0.25f * 1.4426950408889634f : 1.0f;
          v = u32x2{cvtpk(a[0] * sc, a[1] * sc), cvtpk(a[2] * sc, a[3] * sc)};
          u16* dst = (nt == 0 ? ga.o0 : ga.o1) + ((size_t)(head * 64 + (tok >> 5)) * 2 + (g >> 1)) * 256 + (tok & 31) * 8 + (g & 1) * 4;
          st_wt2(dst, v);
        } else {
          const int step = (row0 + wm * 32 + mt * 16) >> 4;
          v = u32x2{cvtpk(a[0], a[1]), cvtpk(a[2], a[3])};
          u16* dst = ga.o2 + ((size_t)(head * 128 + step) * 2 + (g & 1)) * 128 + l15 * 8 + (g >> 1) * 4;
          st_wt2(dst, v);
        }
      } else if constexpr (MODE == 3 || MODE == 5) {
        u32x2 r = res0[mt];
        if constexpr (MODE == 5) {
          f32x2 st = st_lds[lrow];
          unsigned d0 = cvtpk(lo2f(r[0]) - st[0], hi2f(r[0]) - st[0]), d1 = cvtpk(lo2f(r[1]) - st[0], hi2f(r[1]) - st[0]);
          r[0] = cvtpk(lo2f(d0) * st[1], hi2f(d0) * st[1]);
          r[1] = cvtpk(lo2f(d1) * st[1], hi2f(d1) * st[1]);
        }
        unsigned a0 = cvtpk(a[0], a[1]), a1 = cvtpk(a[2], a[3]);
        v = u32x2{cvtpk(lo2f(r[0]) + lo2f(a0), hi2f(r[0]) + hi2f(a0)), cvtpk(lo2f(r[1]) + lo2f(a1), hi2f(r[1]) + hi2f(a1))};
        float f0 = lo2f(v[0]), f1 = hi2f(v[0]), f2 = lo2f(v[1]), f3 = hi2f(v[1]);
        float s_ = (f0 + f1) + (f2 + f3), q_ = fmaf(f0, f0, f1 * f1) + fmaf(f2, f2, f3 * f3);
        s_ = add_x32(add_x16(s_)); q_ = add_x32(add_x16(q_));
        if (g == 0) *(f32x2*)(ga.pout + (size_t)tok * 64 + (cb * 4 + wn) * 2) = f32x2{s_, q_};
      } else if constexpr (MODE == 4 || MODE == 6) {
        unsigned a0 = cvtpk(a[0], a[1]), a1 = cvtpk(a[2], a[3]);
        v = u32x2{cvtpk(gelu_tanh(lo2f(a0)), gelu_tanh(hi2f(a0))), cvtpk(gelu_tanh(lo2f(a1)), gelu_tanh(hi2f(a1)))};
      } else {
        u32x2 x1 = res0[mt], hh = res1[mt];
        unsigned a0 = cvtpk(a[0], a[1]), a1 = cvtpk(a[2], a[3]);
        unsigned b0 = cvtpk(lo2f(x1[0]) + lo2f(a0), hi2f(x1[0]) + hi2f(a0));
        unsigned b1 = cvtpk(lo2f(x1[1]) + lo2f(a1), hi2f(x1[1]) + hi2f(a1));
        v = u32x2{cvtpk(lo2f(hh[0]) + lo2f(b0), hi2f(hh[0]) + hi2f(b0)), cvtpk(lo2f(hh[1]) + lo2f(b1), hi2f(hh[1]) + hi2f(b1))};
      }
      if constexpr (MODE != 1) *(u32x2*)(ost + lrow * C::RSO + nt * 128 + wn * 32 + g * 8) = v;
    }
  }
  if constexpr (MODE != 1) {
    lds_bar();
#pragma unroll
    for (int j = 0; j < NT; ++j) {
      const int c = tid + 512 * j, row = c / (8 * NT), cc = c % (8 * NT);
      const u32x4 v = *(const u32x4*)(ost + row * C::RSO + cc * 16);
      const int tok = row0 + row;
      if constexpr (MODE == 4) {
        const int grp = cb >> 1;
        const int sh = (grp == 0) ? 1 : (grp == 1) ? -1 : (grp == 2) ? 2 : -2;
        const int col = cb * 64 + cc * 8, dt = tok + sh, src = tok - sh;
        if (dt >= 0 && dt < SEQ) st_wt4(ga.o0 + (size_t)dt * DM + col, v);
        if (src < 0 || src >= SEQ) *(u32x4*)(ga.o0 + (size_t)tok * DM + col) = u32x4{0u, 0u, 0u, 0u};
      } else if constexpr (MODE == 6) {
        const int col = cb * 192 + cc * 8;
        if (col < FF) st_wt4(ga.o0 + (size_t)tok * FF + col, v);
      } else {
        st_wt4(ga.o0 + (size_t)tok * DM + cb * 64 + cc * 8, v);
      }
    }
  }
  STAMP(MODE, 3);
}

constexpr int AT_K = 0, AT_V = 65536, AT_ONES = 131072, AT_Q = AT_ONES + 8192, AT_LDS = AT_Q + 8192;

__global__ __launch_bounds__(1024, 4) void attn_kernel(const u16* Qf, const u16* Kf, const u16* Vf, u16* O, const u16* wa,
                                                       const u16* wb, const u16* wc, const u16* wd, const u16* we) {
  extern __shared__ __attribute__((aligned(16))) char lds[];
  STAMP(2, 0);
  const int tid = threadIdx.x, lane = tid & 63;
  const int wid = __builtin_amdgcn_readfirstlane(tid >> 6);
  const int xcd = blockIdx.x & 7, idx = blockIdx.x >> 3;
  const int head = xcd * 4 + (idx >> 3), qb = idx & 7;
  const unsigned lds0 = (unsigned)(uintptr_t)lds;
  const int r = lane & 31, h = lane >> 5;
  const char* kg = (const char*)Kf + (size_t)head * 65536;
  const char* vg = (const char*)Vf + (size_t)head * 65536;
  const int qt = wid & 7, kh = wid >> 3;
  const char* qg = (const char*)Qf + (size_t)head * 65536 + (qb * 8 + qt) * 1024;
  const unsigned lo = lane * 16, pc = (unsigned)(kh * 32768 + qt * 1024);

  if (kh == 0) glds16(qg, lo, lds0 + AT_Q + qt * 1024);
#pragma unroll
  for (int c = 0; c < 2; ++c) {
    glds16(kg, lo + c * 8192 + pc, lds0 + AT_K + c * 8192 + pc);
    glds16(vg, lo + c * 8192 + pc, lds0 + AT_V + c * 8192 + pc);
  }
  *(u32x2*)(lds + AT_ONES + tid * 8) = u32x2{0x3F803F80u, 0x3F803F80u};

  lds_cptr kp = (lds_cptr)lds + AT_K + kh * 32768 + lane * 16;
  lds_cptr vp = (lds_cptr)lds + ((r < 16) ? (AT_V + kh * 32768 + h * 256 + r * 16) : (AT_ONES + (r - 16) * 16));
  const int vstep = (r < 16) ? 4096 : 0;

  f32x16 o, negm, sA, sB;
#pragma unroll
  for (int i = 0; i < 16; ++i) { o[i] = 0.f; negm[i] = 0.f; }

  wait_vm_bar<0>();
  STAMP(2, 1);
#pragma unroll
  for (int c = 2; c < 4; ++c) {
    glds16(kg, lo + c * 8192 + pc, lds0 + AT_K + c * 8192 + pc);
    glds16(vg, lo + c * 8192 + pc, lds0 + AT_V + c * 8192 + pc);
  }
  unsigned tw[5];
  {
    unsigned t = (unsigned)(idx * 1024 + tid) * 128u;
    unsigned ta = t < 512u * 1024u ? t : 0u, tb = t < (unsigned)(DM * FF * 2) ? t : 0u;
    asm volatile("global_load_dword %0, %5, %7\n\tglobal_load_dword %1, %5, %8\n\tglobal_load_dword %2, %5, %9\n\t"
                 "global_load_dword %3, %6, %10\n\tglobal_load_dword %4, %6, %11"
                 : "=&v"(tw[0]), "=&v"(tw[1]), "=&v"(tw[2]), "=&v"(tw[3]), "=&v"(tw[4])
                 : "v"(ta), "v"(tb), "s"(wa), "s"(wb), "s"(wc), "s"(wd), "s"(we)
                 : "memory");
  }
  const bf16x8 qf = lds_rd128((lds_cptr)lds + AT_Q + qt * 1024 + lane * 16);
  bf16x8 kfA, kfB, vA0, vA1, vB0, vB1;
  u32x4 pA0, pA1, pB0, pB1;
  {
    sA = MFMA32(lds_rd128(kp), qf, negm);
    kfA = lds_rd128(kp + 1024);
    float mx = sA[0];
#pragma unroll
    for (int i = 1; i < 16; ++i) mx = fmaxf(mx, sA[i]);
    const float d = max_x32(mx) + 7.0f;
#pragma unroll
    for (int i = 0; i < 16; ++i) { negm[i] = -d; sA[i] -= d; }
    vB0 = lds_rd128(vp); vB1 = vB0;
    pB0 = u32x4{0u, 0u, 0u, 0u}; pB1 = pB0;
  }

#define SBAR() __builtin_amdgcn_sched_barrier(0)
#define PK(x, i) __builtin_amdgcn_perm(__float_as_uint(x[(i) + 1]), __float_as_uint(x[i]), 0x07060302u)
#define EXPS(lo_, hi_) _Pragma("unroll") for (int i = lo_; i < hi_; ++i) sc[i] = __builtin_amdgcn_exp2f(sc[i])
  auto step = [&](f32x16& sc, f32x16& sn, const bf16x8& kf, bf16x8& kfn, const bf16x8& vp0, const bf16x8& vp1,
                  const u32x4& pp0, const u32x4& pp1, bf16x8& vc0, bf16x8& vc1, u32x4& pc0, u32x4& pc1, lds_cptr kc,
                  lds_cptr vc) {
    sn = MFMA32(kf, qf, negm);
    kfn = lds_rd128(kc + 2048);
    vc0 = lds_rd128(vc); vc1 = lds_rd128(vc + 512);
    SBAR();
    EXPS(0, 5);
    SBAR();
    o = MFMA32(vp0, __builtin_bit_cast(bf16x8, pp0), o);
    SBAR();
    EXPS(5, 11);
    SBAR();
    o = MFMA32(vp1, __builtin_bit_cast(bf16x8, pp1), o);
    SBAR();
    EXPS(11, 16);
    SBAR();
#pragma unroll
    for (int i = 0; i < 4; ++i) { pc0[i] = PK(sc, 2 * i); pc1[i] = PK(sc, 8 + 2 * i); }
    unsigned chk = (pc0[0] | pc0[1] | pc0[2]) | (pc0[3] | pc1[0] | pc1[1]) | (pc1[2] | pc1[3]);
    if (__builtin_expect(__any((chk & 0x40004000u) != 0), 0)) {
      f32x16 s2 = MFMA32(lds_rd128(kc), qf, negm);
      float mx = s2[0];
#pragma unroll
      for (int i = 1; i < 16; ++i) mx = fmaxf(mx, s2[i]);
      const float d = fmaxf(max_x32(mx) + 7.0f, 0.f);
      const float alpha = __builtin_amdgcn_exp2f(-d);
#pragma unroll
      for (int i = 0; i < 16; ++i) { o[i] *= alpha; negm[i] -= d; sn[i] -= d; s2[i] = __builtin_amdgcn_exp2f(s2[i] - d); }
#pragma unroll
      for (int i = 0; i < 4; ++i) { pc0[i] = PK(s2, 2 * i); pc1[i] = PK(s2, 8 + 2 * i); }
    }
    SBAR();
  };
  auto group = [&]() {
    step(sA, sB, kfA, kfB, vB0, vB1, pB0, pB1, vA0, vA1, pA0, pA1, kp, vp);
    step(sB, sA, kfB, kfA, vA0, vA1, pA0, pA1, vB0, vB1, pB0, pB1, kp + 1024, vp + 1024);
    step(sA, sB, kfA, kfB, vB0, vB1, pB0, pB1, vA0, vA1, pA0, pA1, kp + 2048, vp + 2048);
    step(sB, sA, kfB, kfA, vA0, vA1, pA0, pA1, vB0, vB1, pB0, pB1, kp + 3072, vp + 3072);
    kp += 4096;
    vp += vstep;
  };
#pragma unroll 1
  for (int it = 0; it < 3; ++it) group();
  asm volatile("s_waitcnt vmcnt(0) lgkmcnt(0)\n\ts_barrier" : "+v"(tw[0]), "+v"(tw[1]), "+v"(tw[2]), "+v"(tw[3]), "+v"(tw[4])::"memory");
#pragma unroll 1
  for (int it = 3; it < 8; ++it) group();
  o = MFMA32(vB0, __builtin_bit_cast(bf16x8, pB0), o);
  o = MFMA32(vB1, __builtin_bit_cast(bf16x8, pB1), o);

  STAMP(2, 2);
  float* mg = (float*)(lds + AT_K) + qt * 640 + lane;
  lds_bar();
  if (kh == 1) {
#pragma unroll
    for (int i = 0; i < 9; ++i) mg[i * 64] = o[i];
    mg[9 * 64] = negm[0];
  }
  lds_bar();
  if (kh == 0) {
    const float n1 = mg[9 * 64], n0 = negm[0];
    const float nm = fminf(n0, n1);
    const float a0 = __builtin_amdgcn_exp2f(nm - n0), a1 = __builtin_amdgcn_exp2f(nm - n1);
#pragma unroll
    for (int i = 0; i < 9; ++i) o[i] = o[i] * a0 + mg[i * 64] * a1;
    const float inv = 1.0f / o[8];
    const int q = qb * 256 + qt * 32 + r;
    u16* dst = O + (size_t)q * DM + head * 16 + 4 * h;
    *(u32x2*)dst = u32x2{cvtpk(o[0] * inv, o[1] * inv), cvtpk(o[2] * inv, o[3] * inv)};
    *(u32x2*)(dst + 8) = u32x2{cvtpk(o[4] * inv, o[5] * inv), cvtpk(o[6] * inv, o[7] * inv)};
  }
  STAMP(2, 3);
}

template <int MODE>
static void launch_gemm(const GArgs& ga, hipStream_t stream) {
  (void)hipFuncSetAttribute((const void*)gemm_kernel<MODE>, hipFuncAttributeMaxDynamicSharedMemorySize, Cfg<MODE>::LDS);
  gemm_kernel<MODE><<<256, 512 + 64 * NLD, Cfg<MODE>::LDS, stream>>>(ga);
}

extern "C" void kernel_launch(void* const* d_in, const int* in_sizes, int n_in, void* d_out, int out_size, void* d_ws,
                              size_t ws_size, hipStream_t stream) {
  const u16* x = (const u16*)d_in[0];
  const u16* wqkv = (const u16*)d_in[1];
  const u16* wo = (const u16*)d_in[2];
  const u16* w1 = (const u16*)d_in[4];
  const u16* w2 = (const u16*)d_in[5];
  const u16* w3 = (const u16*)d_in[6];
  const u16* w4 = (const u16*)d_in[7];
  char* ws = (char*)d_ws;
  u16 *Q = (u16*)(ws + WS_Q), *Kf = (u16*)(ws + WS_K), *Vf = (u16*)(ws + WS_V), *O = (u16*)(ws + WS_O),
      *H = (u16*)(ws + WS_H), *T = (u16*)(ws + WS_T), *X1 = (u16*)(ws + WS_X1), *U = (u16*)(ws + WS_U);
  float *PH = (float*)(ws + WS_PH), *PX = (float*)(ws + WS_PX);
  u16* out = (u16*)d_out;

  launch_gemm<1>(GArgs{x, wqkv, nullptr, nullptr, nullptr, nullptr, Q, Kf, Vf}, stream);
  (void)hipFuncSetAttribute((const void*)attn_kernel, hipFuncAttributeMaxDynamicSharedMemorySize, AT_LDS);
  attn_kernel<<<256, 1024, AT_LDS, stream>>>(Q, Kf, Vf, O, wo, w1, w2, w3, w4);
  launch_gemm<3>(GArgs{O, wo, x, nullptr, nullptr, PH, H, nullptr, nullptr}, stream);
  launch_gemm<4>(GArgs{H, w1, nullptr, nullptr, PH, nullptr, T, nullptr, nullptr}, stream);
  launch_gemm<5>(GArgs{T, w2, H, nullptr, PH, PX, X1, nullptr, nullptr}, stream);
  launch_gemm<6>(GArgs{X1, w3, nullptr, nullptr, PX, nullptr, U, nullptr, nullptr}, stream);
  launch_gemm<7>(GArgs{U, w4, X1, H, nullptr, nullptr, out, nullptr, nullptr}, stream);
}
```

```cpp
#include <hip/hip_runtime.h>
#include <hip/hip_bf16.h>
#include <stdint.h>

typedef unsigned short u16;
typedef __attribute__((ext_vector_type(8))) short bf16x8;
typedef __attribute__((ext_vector_type(4))) short s16x4;
typedef __attribute__((ext_vector_type(2))) float f32x2;
typedef __attribute__((ext_vector_type(4))) float f32x4;
typedef __attribute__((ext_vector_type(16))) float f32x16;
typedef __attribute__((ext_vector_type(2))) __bf16 bf16x2_t;
typedef __attribute__((ext_vector_type(4))) unsigned u32x4;
typedef __attribute__((ext_vector_type(2))) unsigned u32x2;
typedef short v4i16_t __attribute__((ext_vector_type(4)));
typedef __attribute__((address_space(3))) const char* lds_cptr;

#define DEV __device__ __forceinline__
#define MFMA16(a, b, c) __builtin_amdgcn_mfma_f32_16x16x32_bf16((a), (b), (c), 0, 0, 0)
#define MFMA32(a, b, c) __builtin_amdgcn_mfma_f32_32x32x16_bf16((a), (b), (c), 0, 0, 0)

constexpr int SEQ = 2048, DM = 512, NH = 32, FF = 1376;

constexpr size_t WS_Q = 0, WS_K = 2u << 20, WS_V = 4u << 20, WS_O = 6u << 20, WS_H = 8u << 20, WS_PH = 10u << 20,
                 WS_PX = 11u << 20, WS_T = 12u << 20, WS_X1 = 14u << 20, WS_U = 16u << 20;

DEV unsigned cvtpk(float lo, float hi) {
  f32x2 v = {lo, hi};
  bf16x2_t b = __builtin_convertvector(v, bf16x2_t);
  return __builtin_bit_cast(unsigned, b);
}
DEV float lo2f(unsigned p) { return __uint_as_float(p << 16); }
DEV float hi2f(unsigned p) { return __uint_as_float(p & 0xffff0000u); }
DEV float rbf(float x) { return lo2f(cvtpk(x, x)); }

DEV void glds16(const void* gbase, unsigned voff, unsigned lds_base) {
  unsigned sv;
  asm volatile("s_mov_b32 %0, m0\n\ts_mov_b32 m0, %3\n\ts_nop 0\n\tglobal_load_lds_dwordx4 %1, %2\n\ts_mov_b32 m0, %0"
               : "=&s"(sv)
               : "v"(voff), "s"(gbase), "s"(lds_base)
               : "memory");
}
DEV u32x4 aload4(const void* gbase, unsigned voff) {
  u32x4 r;
  asm volatile("global_load_dwordx4 %0, %1, %2" : "=v"(r) : "v"(voff), "s"(gbase) : "memory");
  return r;
}
DEV u32x2 aload2(const void* gbase, unsigned voff) {
  u32x2 r;
  asm volatile("global_load_dwordx2 %0, %1, %2" : "=v"(r) : "v"(voff), "s"(gbase) : "memory");
  return r;
}
DEV void st_wt4(void* p, u32x4 v) { asm volatile("global_store_dwordx4 %0, %1, off sc1\n\ts_nop 1" ::"v"(p), "v"(v) : "memory"); }
DEV void st_wt2(void* p, u32x2 v) { asm volatile("global_store_dwordx2 %0, %1, off sc1\n\ts_nop 1" ::"v"(p), "v"(v) : "memory"); }
template <class T>
DEV void pin(T& x) { asm volatile("" : "+v"(x)); }
template <int N>
DEV void wait_vm_bar() {
  asm volatile("s_waitcnt vmcnt(%0) lgkmcnt(0)\n\ts_barrier" ::"i"(N) : "memory");
}
DEV void lds_bar() { asm volatile("s_waitcnt lgkmcnt(0)\n\ts_barrier" ::: "memory"); }
DEV bf16x8 lds_rd128(lds_cptr p) { return *(const __attribute__((address_space(3))) bf16x8*)p; }
DEV s16x4 lds_tr(lds_cptr p) {
  return __builtin_bit_cast(s16x4, __builtin_amdgcn_ds_read_tr16_b64_v4i16((__attribute__((address_space(3))) v4i16_t*)p));
}
template <int CTRL>
DEV float dpp_add(float v) {
  int x = __builtin_amdgcn_update_dpp(0, __float_as_int(v), CTRL, 0xf, 0xf, true);
  return v + __int_as_float(x);
}
DEV float add_x16(float v) {
  auto p = __builtin_amdgcn_permlane16_swap(__float_as_uint(v), __float_as_uint(v), false, false);
  return __uint_as_float(p[0]) + __uint_as_float(p[1]);
}
DEV float add_x32(float v) {
  auto p = __builtin_amdgcn_permlane32_swap(__float_as_uint(v), __float_as_uint(v), false, false);
  return __uint_as_float(p[0]) + __uint_as_float(p[1]);
}
DEV float max_x32(float v) {
  auto p = __builtin_amdgcn_permlane32_swap(__float_as_uint(v), __float_as_uint(v), false, false);
  return fmaxf(__uint_as_float(p[0]), __uint_as_float(p[1]));
}
DEV float wave_sum(float v) {
  v = dpp_add<0x121>(v);
  v = dpp_add<0x122>(v);
  v = dpp_add<0x124>(v);
  v = dpp_add<0x128>(v);
  return add_x32(add_x16(v));
}
DEV float dot2_ones(unsigned p, float acc) {
  return __builtin_amdgcn_fdot2_f32_bf16(__builtin_bit_cast(bf16x2_t, p), __builtin_bit_cast(bf16x2_t, 0x3F803F80u), acc, false);
}
DEV float dot2u(unsigned a, unsigned b, float acc) {
  return __builtin_amdgcn_fdot2_f32_bf16(__builtin_bit_cast(bf16x2_t, a), __builtin_bit_cast(bf16x2_t, b), acc, false);
}
DEV float gelu_tanh(float x) {
  const float c = -2.0f * 0.7978845608028654f * 1.4426950408889634f;
  float u = x * (1.0f + 0.044715f * x * x);
  float e = __builtin_amdgcn_exp2f(c * u);
  return x * __builtin_amdgcn_rcpf(1.0f + e);
}
DEV int bswz(int k) { return ((k >> 1) & 1) | (((k >> 3) & 1) << 1); }

#define STAMP(kid, i) do { } while (0)
#define DBG(kid, w, i) do { } while (0)

template <int I, int N, class F>
DEV void static_for(F&& f) {
  if constexpr (I < N) {
    f(std::integral_constant<int, I>{});
    static_for<I + 1, N>(f);
  }
}

DEV void ln_consts(float s, float q, float& mb, float& sd, float& m, float& v) {
  m = s * (1.0f / DM);
  v = fmaxf(q * (1.0f / DM) - m * m, 0.f);
  mb = rbf(m);
  sd = rbf(__builtin_sqrtf(rbf(rbf(v) + 1e-5f)));
}

constexpr int NLD = 4;
template <int MODE>
struct Cfg {
  static constexpr int NT = (MODE == 1 || MODE == 6) ? 3 : 1;
  static constexpr bool LN = (MODE == 1 || MODE == 4 || MODE == 6);
  static constexpr bool PST = (MODE == 4 || MODE == 5 || MODE == 6);
  static constexpr int K = (MODE == 7) ? FF : DM;
  static constexpr int LDA = K;
  static constexpr int LDW = (MODE == 1) ? 3 * DM : (MODE == 6 ? FF : DM);
  static constexpr int NSTAGE = (K + 63) / 64;
  static constexpr int SPB = (NT == 1) ? 2 : 1;
  static constexpr int NB = (NSTAGE + SPB - 1) / SPB;
  static constexpr int RS = 128 * NT;
  static constexpr int BBYTES = 64 * RS;
  static constexpr int SLOT = BBYTES + 8192;
  static constexpr int NGSLOT = 4;
  static constexpr int NSLOT = NGSLOT * SPB;
  static constexpr int DEPTH = NGSLOT - 1;
  static constexpr int X_ST = NSLOT * SLOT;
  static constexpr int X_CS = X_ST + 512;
  static constexpr int X_PX = X_CS + 1536;
  static constexpr int X_OUT = X_PX + 2048;
  static constexpr int RSO = 128 * NT + 16;
  static constexpr int LDS = X_OUT + (MODE == 1 ? 0 : 64 * RSO);
  static constexpr int LPS = 8 * (NT + 1) / NLD;
};

struct GArgs {
  const u16* A;
  const u16* W;
  const u16* r0;
  const u16* r1;
  const float* pin_;
  float* pout;
  u16* o0;
  u16* o1;
  u16* o2;
};

template <int MODE>
__global__ __launch_bounds__(512 + 64 * NLD, 2) void gemm_kernel(GArgs ga) {
  using C = Cfg<MODE>;
  constexpr int NT = C::NT;
  extern __shared__ __attribute__((aligned(16))) char lds[];
  STAMP(MODE, 0);
  const int tid = threadIdx.x, lane = tid & 63;
  const int wid = __builtin_amdgcn_readfirstlane(tid >> 6);
  const int xcd = blockIdx.x & 7, idx = blockIdx.x >> 3;
  const int rb = xcd * 4 + (idx >> 3), cb = idx & 7;
  const int row0 = rb * 64;
  const unsigned lds0 = (unsigned)(uintptr_t)lds;

  if (wid >= 8) {
    const int L = wid - 8;
    constexpr int IA = 8 / NLD, IB = 8 * NT / NLD;
    unsigned boff[IB];
    int bk[IB];
#pragma unroll
    for (int j = 0; j < IB; ++j) {
      int o = (L * IB + j) * 1024 + lane * 16;
      int k = o / C::RS, rem = o % C::RS;
      int grp = rem >> 7, blk = (rem >> 5) & 3, hf = (rem >> 4) & 1;
      int col;
      if (MODE == 1) col = grp * 512 + cb * 64;
      else col = cb * (64 * NT) + grp * 64;
      col += ((blk ^ bswz(k)) * 16) + hf * 8;
      if (MODE == 6 && col > FF - 8) col = FF - 8;
      bk[j] = k;
      boff[j] = (unsigned)(k * C::LDW + col) * 2u;
    }
    unsigned aoffj[IA];
#pragma unroll
    for (int j = 0; j < IA; ++j) {
      int r = (L * IA + j) * 8 + (lane >> 3), c = (lane & 7) ^ ((r >> 1) & 7);
      aoffj[j] = (unsigned)((row0 + r) * C::LDA + c * 8) * 2u;
    }
    auto issue_sub = [&](int s) {
      const unsigned slot = lds0 + (unsigned)((s % C::NSLOT) * C::SLOT);
#pragma unroll
      for (int j = 0; j < IA; ++j) glds16(ga.A, aoffj[j] + s * 128, slot + C::BBYTES + (L * IA + j) * 1024);
#pragma unroll
      for (int j = 0; j < IB; ++j) {
        unsigned off = boff[j] + (unsigned)(s * 64 * C::LDW) * 2u;
        if (MODE == 7 && s == C::NSTAGE - 1 && s * 64 + bk[j] >= FF) off = boff[j] + (unsigned)((FF - 64) * C::LDW) * 2u;
        glds16(ga.W, off, slot + (unsigned)(L * IB + j) * 1024u);
      }
    };
    auto issue_group = [&](auto g_) {
      constexpr int gg = g_.value;
      static_for<0, C::SPB>([&](auto u_) { if constexpr (gg < C::NB && gg * C::SPB + u_.value < C::NSTAGE) issue_sub(gg * C::SPB + u_.value); });
    };
    issue_group(std::integral_constant<int, 0>{});
    issue_group(std::integral_constant<int, 1>{});
    static_for<0, C::NB>([&](auto b_) {
      constexpr int b = b_.value;
      constexpr int lastg0 = (b == 0) ? 1 : b + C::DEPTH - 1;
      constexpr int lastg = lastg0 < C::NB - 1 ? lastg0 : C::NB - 1;
      constexpr int sub_after = ((lastg + 1) * C::SPB > C::NSTAGE ? C::NSTAGE : (lastg + 1) * C::SPB) - ((b + 1) * C::SPB > C::NSTAGE ? C::NSTAGE : (b + 1) * C::SPB);
      asm volatile("s_waitcnt vmcnt(%0)" ::"i"(sub_after * C::LPS) : "memory");
      if (wid == 8) DBG(MODE, 1, b);
      wait_vm_bar<sub_after * C::LPS>();
      if constexpr (b == 0) {
        static_for<2, C::DEPTH + 1>([&](auto g_) { issue_group(g_); });
      } else {
        issue_group(std::integral_constant<int, b + C::DEPTH>{});
      }
    });
    return;
  }

  const int wm = wid & 1, wn = wid >> 1;
  const int l15 = lane & 15, g = lane >> 4;
  u32x4 ps[2];
  u32x2 res0[2], res1[2];
  if constexpr (C::PST) {
    const float* pp = ga.pin_ + (size_t)(row0 + (tid >> 3)) * 64 + (tid & 7) * 8;
    ps[0] = *(const u32x4*)pp;
    ps[1] = *(const u32x4*)(pp + 4);
  }
  if constexpr (MODE == 3 || MODE == 5 || MODE == 7) {
#pragma unroll
    for (int mt = 0; mt < 2; ++mt) {
      size_t o = (size_t)(row0 + wm * 32 + mt * 16 + l15) * DM + cb * 64 + wn * 16 + 4 * g;
      res0[mt] = *(const u32x2*)(ga.r0 + o);
      if constexpr (MODE == 7) res1[mt] = *(const u32x2*)(ga.r1 + o);
    }
  }

  int xoff[2][2];
#pragma unroll
  for (int mt = 0; mt < 2; ++mt)
#pragma unroll
    for (int kk = 0; kk < 2; ++kk) {
      int r = wm * 32 + mt * 16 + l15;
      xoff[mt][kk] = C::BBYTES + r * 128 + (((4 * kk + g) ^ ((r >> 1) & 7)) * 16);
    }
  int woff[2][2];
  {
    int q = l15 >> 2, p = l15 & 3;
#pragma unroll
    for (int kk = 0; kk < 2; ++kk)
#pragma unroll
      for (int t = 0; t < 2; ++t) {
        int k = 32 * kk + 8 * g + 4 * t + q;
        woff[kk][t] = k * C::RS + ((wn ^ bswz(k)) * 32) + p * 8;
      }
  }

  f32x4 acc[2][NT];
  f32x4 cs[NT];
  const bf16x8 ones8 = {0x3F80, 0x3F80, 0x3F80, 0x3F80, 0x3F80, 0x3F80, 0x3F80, 0x3F80};
#pragma unroll
  for (int nt = 0; nt < NT; ++nt) {
    cs[nt] = f32x4{0.f, 0.f, 0.f, 0.f};
#pragma unroll
    for (int mt = 0; mt < 2; ++mt) acc[mt][nt] = f32x4{0.f, 0.f, 0.f, 0.f};
  }
  f32x2* st_lds = (f32x2*)(lds + C::X_ST);

    {
      if constexpr (C::PST) {
        float s_ = (__uint_as_float(ps[0][0]) + __uint_as_float(ps[0][2])) + (__uint_as_float(ps[1][0]) + __uint_as_float(ps[1][2]));
        float q_ = (__uint_as_float(ps[0][1]) + __uint_as_float(ps[0][3])) + (__uint_as_float(ps[1][1]) + __uint_as_float(ps[1][3]));
        s_ = dpp_add<0xB1>(s_); q_ = dpp_add<0xB1>(q_);
        s_ = dpp_add<0x4E>(s_); q_ = dpp_add<0x4E>(q_);
        s_ = dpp_add<0x141>(s_); q_ = dpp_add<0x141>(q_);
        float mb, sd, m, v;
        ln_consts(s_, q_, mb, sd, m, v);
        float M = mb, R = 1.0f / sd;
        if constexpr (MODE == 4) {
          float m2 = (m - mb) * R, v2 = v * R * R;
          float m2b = rbf(m2);
          float sd2 = rbf(__builtin_sqrtf(rbf(rbf(v2) + 1e-5f)));
          M = mb + m2b * sd;
          R = R / sd2;
        }
        if ((tid & 7) == 0) st_lds[tid >> 3] = f32x2{M, R};
      }
    }
  f32x4 sx[2] = {f32x4{0.f, 0.f, 0.f, 0.f}, f32x4{0.f, 0.f, 0.f, 0.f}}, qx[2] = {sx[0], sx[0]};
  static_for<0, C::NB>([&](auto b_) {
    constexpr int s = b_.value;
    if (wid == 0) DBG(MODE, 2, s);
    lds_bar();
    if (wid == 0) DBG(MODE, 0, s);
    if constexpr (s == 0) STAMP(MODE, 1);
    constexpr int S0 = s * C::SPB;
    constexpr int NSUB = ((S0 + C::SPB > C::NSTAGE) ? C::NSTAGE : S0 + C::SPB) - S0;
    constexpr int NKS = 2 * NSUB - ((MODE == 7 && S0 + NSUB == C::NSTAGE) ? 1 : 0);
    bf16x8 xf[NKS][2], wf[NKS][NT];
#pragma unroll
    for (int ks = 0; ks < NKS; ++ks) {
      const lds_cptr bs = (lds_cptr)lds + ((S0 + (ks >> 1)) % C::NSLOT) * C::SLOT;
      const int kk = ks & 1;
#pragma unroll
      for (int nt = 0; nt < NT; ++nt) {
        s16x4 w0 = lds_tr(bs + woff[kk][0] + nt * 128);
        s16x4 w1 = lds_tr(bs + woff[kk][1] + nt * 128);
        wf[ks][nt] = __builtin_shufflevector(w0, w1, 0, 1, 2, 3, 4, 5, 6, 7);
        if (nt == 0) {
#pragma unroll
          for (int mt = 0; mt < 2; ++mt) xf[ks][mt] = lds_rd128(bs + xoff[mt][kk]);
        }
      }
    }
#pragma unroll
    for (int ks = 0; ks < NKS; ++ks) {
#pragma unroll
      for (int nt = 0; nt < NT; ++nt) {
#pragma unroll
        for (int mt = 0; mt < 2; ++mt) {
          if (MODE == 1 && nt == 2) acc[mt][nt] = MFMA16(xf[ks][mt], wf[ks][nt], acc[mt][nt]);
          else acc[mt][nt] = MFMA16(wf[ks][nt], xf[ks][mt], acc[mt][nt]);
        }
      }
    }
    if constexpr (C::LN) {
      auto colsum = [&](auto par_) {
#pragma unroll
        for (int ks = par_.value; ks < NKS; ks += 2)
#pragma unroll
          for (int nt = 0; nt < NT; ++nt) {
            if (MODE == 1 && nt == 2) cs[nt] = MFMA16(ones8, wf[ks][nt], cs[nt]);
            else cs[nt] = MFMA16(wf[ks][nt], ones8, cs[nt]);
          }
      };
      if (wm == 0) colsum(std::integral_constant<int, 0>{});
      else colsum(std::integral_constant<int, 1>{});
    }
    if constexpr (MODE == 1) {
      auto stat = [&](const bf16x8 (&xx)[2]) {
#pragma unroll
        for (int mt = 0; mt < 2; ++mt) {
          sx[mt] = MFMA16(ones8, xx[mt], sx[mt]);
          qx[mt] = MFMA16(xx[mt], xx[mt], qx[mt]);
        }
      };
      if (wn == ((2 * s) & 3)) stat(xf[0]);
      else if (wn == ((2 * s + 1) & 3)) stat(xf[1]);
    }
    __builtin_amdgcn_sched_barrier(0);
  });

  float* cs_lds = (float*)(lds + C::X_CS);
  if constexpr (C::LN) {
#pragma unroll
    for (int nt = 0; nt < NT; ++nt) {
      float* d = cs_lds + ((wm * 4 + wn) * NT + nt) * 16;
      if (MODE == 1 && nt == 2) { if (g == 0) d[l15] = cs[nt][0]; }
      else if (l15 == 0) *(f32x4*)(d + 4 * g) = cs[nt];
    }
    if constexpr (MODE == 1) {
      f32x2* px = (f32x2*)(lds + C::X_PX);
      const int di = l15 & 3;
      float b0 = di == 0 ? qx[0][0] : di == 1 ? qx[0][1] : di == 2 ? qx[0][2] : qx[0][3];
      float b1 = di == 0 ? qx[1][0] : di == 1 ? qx[1][1] : di == 2 ? qx[1][2] : qx[1][3];
      if (g == (l15 >> 2)) { px[wn * 64 + wm * 32 + l15] = f32x2{sx[0][0], b0}; px[wn * 64 + wm * 32 + 16 + l15] = f32x2{sx[1][0], b1}; }
      lds_bar();
      if (tid < 64) {
        f32x2 t = (px[tid] + px[64 + tid]) + (px[128 + tid] + px[192 + tid]);
        float mb, sd, m, v;
        ln_consts(t[0], t[1], mb, sd, m, v);
        st_lds[tid] = f32x2{mb, 1.0f / sd};
      }
    }
    lds_bar();
#pragma unroll
    for (int nt = 0; nt < NT; ++nt) {
      const float* d = cs_lds + (((1 - wm) * 4 + wn) * NT + nt) * 16;
      if (MODE == 1 && nt == 2) cs[nt][0] += d[l15];
      else cs[nt] += *(const f32x4*)(d + 4 * g);
    }
  }
  STAMP(MODE, 2);
  char* ost = lds + C::X_OUT;
#pragma unroll
  for (int mt = 0; mt < 2; ++mt) {
    const int lrow = wm * 32 + mt * 16 + l15;
    const int tok = row0 + lrow;
#pragma unroll
    for (int nt = 0; nt < NT; ++nt) {
      f32x4 a = acc[mt][nt];
      if constexpr (C::LN) {
        if (MODE == 1 && nt == 2) {
          float c = cs[nt][0];
#pragma unroll
          for (int i = 0; i < 4; ++i) {
            f32x2 st = st_lds[wm * 32 + mt * 16 + 4 * g + i];
            a[i] = (a[i] - st[0] * c) * st[1];
          }
        } else {
          f32x4 c = cs[nt];
          f32x2 st = st_lds[lrow];
#pragma unroll
          for (int i = 0; i < 4; ++i) a[i] = (a[i] - st[0] * c[i]) * st[1];
        }
      }
      u32x2 v;
      if constexpr (MODE == 1) {
        const int head = cb * 4 + wn;
        if (nt < 2) {
          float sc = (nt == 0) ? 0.25f * 1.4426950408889634f : 1.0f;
          v = u32x2{cvtpk(a[0] * sc, a[1] * sc), cvtpk(a[2] * sc, a[3] * sc)};
          u16* dst = (nt == 0 ? ga.o0 : ga.o1) + ((size_t)(head * 64 + (tok >> 5)) * 2 + (g >> 1)) * 256 + (tok & 31) * 8 + (g & 1) * 4;
          st_wt2(dst, v);
        } else {
          const int step = (row0 + wm * 32 + mt * 16) >> 4;
          v = u32x2{cvtpk(a[0], a[1]), cvtpk(a[2], a[3])};
          u16* dst = ga.o2 + ((size_t)(head * 128 + step) * 2 + (g & 1)) * 128 + l15 * 8 + (g >> 1) * 4;
          st_wt2(dst, v);
        }
      } else if constexpr (MODE == 3 || MODE == 5) {
        u32x2 r = res0[mt];
        if constexpr (MODE == 5) {
          f32x2 st = st_lds[lrow];
          unsigned d0 = cvtpk(lo2f(r[0]) - st[0], hi2f(r[0]) - st[0]), d1 = cvtpk(lo2f(r[1]) - st[0], hi2f(r[1]) - st[0]);
          r[0] = cvtpk(lo2f(d0) * st[1], hi2f(d0) * st[1]);
          r[1] = cvtpk(lo2f(d1) * st[1], hi2f(d1) * st[1]);
        }
        unsigned a0 = cvtpk(a[0], a[1]), a1 = cvtpk(a[2], a[3]);
        v = u32x2{cvtpk(lo2f(r[0]) + lo2f(a0), hi2f(r[0]) + hi2f(a0)), cvtpk(lo2f(r[1]) + lo2f(a1), hi2f(r[1]) + hi2f(a1))};
        float f0 = lo2f(v[0]), f1 = hi2f(v[0]), f2 = lo2f(v[1]), f3 = hi2f(v[1]);
        float s_ = (f0 + f1) + (f2 + f3), q_ = fmaf(f0, f0, f1 * f1) + fmaf(f2, f2, f3 * f3);
        s_ = add_x32(add_x16(s_)); q_ = add_x32(add_x16(q_));
        if (g == 0) *(f32x2*)(ga.pout + (size_t)tok * 64 + (cb * 4 + wn) * 2) = f32x2{s_, q_};
      } else if constexpr (MODE == 4 || MODE == 6) {
        unsigned a0 = cvtpk(a[0], a[1]), a1 = cvtpk(a[2], a[3]);
        v = u32x2{cvtpk(gelu_tanh(lo2f(a0)), gelu_tanh(hi2f(a0))), cvtpk(gelu_tanh(lo2f(a1)), gelu_tanh(hi2f(a1)))};
      } else {
        u32x2 x1 = res0[mt], hh = res1[mt];
        unsigned a0 = cvtpk(a[0], a[1]), a1 = cvtpk(a[2], a[3]);
        unsigned b0 = cvtpk(lo2f(x1[0]) + lo2f(a0), hi2f(x1[0]) + hi2f(a0));
        unsigned b1 = cvtpk(lo2f(x1[1]) + lo2f(a1), hi2f(x1[1]) + hi2f(a1));
        v = u32x2{cvtpk(lo2f(hh[0]) + lo2f(b0), hi2f(hh[0]) + hi2f(b0)), cvtpk(lo2f(hh[1]) + lo2f(b1), hi2f(hh[1]) + hi2f(b1))};
      }
      if constexpr (MODE != 1) *(u32x2*)(ost + lrow * C::RSO + nt * 128 + wn * 32 + g * 8) = v;
    }
  }
  if constexpr (MODE != 1) {
    lds_bar();
#pragma unroll
    for (int j = 0; j < NT; ++j) {
      const int c = tid + 512 * j, row = c / (8 * NT), cc = c % (8 * NT);
      const u32x4 v = *(const u32x4*)(ost + row * C::RSO + cc * 16);
      const int tok = row0 + row;
      if constexpr (MODE == 4) {
        const int grp = cb >> 1;
        const int sh = (grp == 0) ? 1 : (grp == 1) ? -1 : (grp == 2) ? 2 : -2;
        const int col = cb * 64 + cc * 8, dt = tok + sh, src = tok - sh;
        if (dt >= 0 && dt < SEQ) st_wt4(ga.o0 + (size_t)dt * DM + col, v);
        if (src < 0 || src >= SEQ) *(u32x4*)(ga.o0 + (size_t)tok * DM + col) = u32x4{0u, 0u, 0u, 0u};
      } else if constexpr (MODE == 6) {
        const int col = cb * 192 + cc * 8;
        if (col < FF) st_wt4(ga.o0 + (size_t)tok * FF + col, v);
      } else {
        st_wt4(ga.o0 + (size_t)tok * DM + cb * 64 + cc * 8, v);
      }
    }
  }
  STAMP(MODE, 3);
}

constexpr int AT_K = 0, AT_V = 65536, AT_ONES = 131072, AT_Q = AT_ONES + 8192, AT_LDS = AT_Q + 8192;

__global__ __launch_bounds__(640, 2) void attn_kernel(const u16* Qf, const u16* Kf, const u16* Vf, u16* O, const u16* wa,
                                                       const u16* wb, const u16* wc, const u16* wd, const u16* we) {
  extern __shared__ __attribute__((aligned(16))) char lds[];
  STAMP(2, 0);
  const int tid = threadIdx.x, lane = tid & 63;
  const int wid = __builtin_amdgcn_readfirstlane(tid >> 6);
  const int xcd = blockIdx.x & 7, idx = blockIdx.x >> 3;
  const int head = xcd * 4 + (idx >> 3), qb = idx & 7;
  const unsigned lds0 = (unsigned)(uintptr_t)lds;
  const unsigned lo = lane * 16;

  if (wid >= 8) {
    const int L = wid - 8;
    const char* src = (const char*)(L ? Vf : Kf) + (size_t)head * 65536;
    const char* qg = (const char*)Qf + (size_t)head * 65536 + (qb * 8 + L * 4) * 1024;
    const unsigned dst = lds0 + (L ? AT_V : AT_K);
#pragma unroll
    for (int j = 0; j < 4; ++j) glds16(qg, lo + j * 1024, lds0 + AT_Q + (L * 4 + j) * 1024);
#pragma unroll
    for (int j = 0; j < 16; ++j) glds16(src, lo + j * 1024, dst + j * 1024);
    wait_vm_bar<0>();
#pragma unroll
    for (int j = 16; j < 64; ++j) glds16(src, lo + j * 1024, dst + j * 1024);
    unsigned tw[5][3];
#pragma unroll
    for (int j = 0; j < 3; ++j) {
      unsigned t = (unsigned)((idx * 2 + L) * 192 + j * 64 + lane) * 128u;
      unsigned ta = t < 512u * 1024u ? t : 0u, tb = t < (unsigned)(DM * FF * 2) ? t : 0u;
      asm volatile("global_load_dword %0, %5, %7\n\tglobal_load_dword %1, %5, %8\n\tglobal_load_dword %2, %5, %9\n\t"
                   "global_load_dword %3, %6, %10\n\tglobal_load_dword %4, %6, %11"
                   : "=&v"(tw[0][j]), "=&v"(tw[1][j]), "=&v"(tw[2][j]), "=&v"(tw[3][j]), "=&v"(tw[4][j])
                   : "v"(ta), "v"(tb), "s"(wa), "s"(wb), "s"(wc), "s"(wd), "s"(we)
                   : "memory");
    }
    asm volatile("s_waitcnt vmcnt(15)\n\ts_barrier\n\ts_waitcnt vmcnt(0)"
                 : "+v"(tw[0][0]), "+v"(tw[1][0]), "+v"(tw[2][0]), "+v"(tw[3][0]), "+v"(tw[4][0]), "+v"(tw[0][1]), "+v"(tw[1][1]),
                   "+v"(tw[2][1]), "+v"(tw[3][1]), "+v"(tw[4][1]), "+v"(tw[0][2]), "+v"(tw[1][2]), "+v"(tw[2][2]), "+v"(tw[3][2]),
                   "+v"(tw[4][2])::"memory");
    return;
  }
  const int r = lane & 31, h = lane >> 5;
  const int qt = wid;
  *(u32x4*)(lds + AT_ONES + tid * 16) = u32x4{0x3F803F80u, 0x3F803F80u, 0x3F803F80u, 0x3F803F80u};

  lds_cptr kp = (lds_cptr)lds + AT_K + lane * 16;
  lds_cptr vp = (lds_cptr)lds + ((r < 16) ? (AT_V + h * 256 + r * 16) : (AT_ONES + (r - 16) * 16));
  const int vstep = (r < 16) ? 4096 : 0;

  f32x16 o, negm, sA, sB;
#pragma unroll
  for (int i = 0; i < 16; ++i) { o[i] = 0.f; negm[i] = 0.f; }

  lds_bar();
  STAMP(2, 1);
  const bf16x8 qf = lds_rd128((lds_cptr)lds + AT_Q + qt * 1024 + lane * 16);
  bf16x8 kfA, kfB, vA0, vA1, vB0, vB1;
  u32x4 pA0, pA1, pB0, pB1;
  {
    sA = MFMA32(lds_rd128(kp), qf, negm);
    kfA = lds_rd128(kp + 1024);
    float mx = sA[0];
#pragma unroll
    for (int i = 1; i < 16; ++i) mx = fmaxf(mx, sA[i]);
    const float d = max_x32(mx) + 7.0f;
#pragma unroll
    for (int i = 0; i < 16; ++i) { negm[i] = -d; sA[i] -= d; }
    vB0 = lds_rd128(vp); vB1 = vB0;
    pB0 = u32x4{0u, 0u, 0u, 0u}; pB1 = pB0;
  }

#define SBAR() __builtin_amdgcn_sched_barrier(0)
#define PK(x, i) __builtin_amdgcn_perm(__float_as_uint(x[(i) + 1]), __float_as_uint(x[i]), 0x07060302u)
#define EXPS(lo_, hi_) _Pragma("unroll") for (int i = lo_; i < hi_; ++i) sc[i] = __builtin_amdgcn_exp2f(sc[i])
  auto step = [&](f32x16& sc, f32x16& sn, const bf16x8& kf, bf16x8& kfn, const bf16x8& vp0, const bf16x8& vp1,
                  const u32x4& pp0, const u32x4& pp1, bf16x8& vc0, bf16x8& vc1, u32x4& pc0, u32x4& pc1, lds_cptr kc,
                  lds_cptr vc) {
    sn = MFMA32(kf, qf, negm);
    kfn = lds_rd128(kc + 2048);
    vc0 = lds_rd128(vc); vc1 = lds_rd128(vc + 512);
    SBAR();
    EXPS(0, 5);
    SBAR();
    o = MFMA32(vp0, __builtin_bit_cast(bf16x8, pp0), o);
    SBAR();
    EXPS(5, 11);
    SBAR();
    o = MFMA32(vp1, __builtin_bit_cast(bf16x8, pp1), o);
    SBAR();
    EXPS(11, 16);
    SBAR();
#pragma unroll
    for (int i = 0; i < 4; ++i) { pc0[i] = PK(sc, 2 * i); pc1[i] = PK(sc, 8 + 2 * i); }
    unsigned chk = (pc0[0] | pc0[1] | pc0[2]) | (pc0[3] | pc1[0] | pc1[1]) | (pc1[2] | pc1[3]);
    if (__builtin_expect(__any((chk & 0x40004000u) != 0), 0)) {
      f32x16 s2 = MFMA32(lds_rd128(kc), qf, negm);
      float mx = s2[0];
#pragma unroll
      for (int i = 1; i < 16; ++i) mx = fmaxf(mx, s2[i]);
      const float d = fmaxf(max_x32(mx) + 7.0f, 0.f);
      const float alpha = __builtin_amdgcn_exp2f(-d);
#pragma unroll
      for (int i = 0; i < 16; ++i) { o[i] *= alpha; negm[i] -= d; sn[i] -= d; s2[i] = __builtin_amdgcn_exp2f(s2[i] - d); }
#pragma unroll
      for (int i = 0; i < 4; ++i) { pc0[i] = PK(s2, 2 * i); pc1[i] = PK(s2, 8 + 2 * i); }
    }
    SBAR();
  };
  auto group = [&]() {
    step(sA, sB, kfA, kfB, vB0, vB1, pB0, pB1, vA0, vA1, pA0, pA1, kp, vp);
    step(sB, sA, kfB, kfA, vA0, vA1, pA0, pA1, vB0, vB1, pB0, pB1, kp + 1024, vp + 1024);
    step(sA, sB, kfA, kfB, vB0, vB1, pB0, pB1, vA0, vA1, pA0, pA1, kp + 2048, vp + 2048);
    step(sB, sA, kfB, kfA, vA0, vA1, pA0, pA1, vB0, vB1, pB0, pB1, kp + 3072, vp + 3072);
    kp += 4096;
    vp += vstep;
  };
#pragma unroll 1
  for (int it = 0; it < 3; ++it) group();
  lds_bar();
#pragma unroll 1
  for (int it = 3; it < 16; ++it) group();
  o = MFMA32(vB0, __builtin_bit_cast(bf16x8, pB0), o);
  o = MFMA32(vB1, __builtin_bit_cast(bf16x8, pB1), o);

  STAMP(2, 2);
  {
    const float inv = 1.0f / o[8];
    const int q = qb * 256 + qt * 32 + r;
    u16* dst = O + (size_t)q * DM + head * 16 + 4 * h;
    *(u32x2*)dst = u32x2{cvtpk(o[0] * inv, o[1] * inv), cvtpk(o[2] * inv, o[3] * inv)};
    *(u32x2*)(dst + 8) = u32x2{cvtpk(o[4] * inv, o[5] * inv), cvtpk(o[6] * inv, o[7] * inv)};
  }
  STAMP(2, 3);
}

template <int MODE>
static void launch_gemm(const GArgs& ga, hipStream_t stream) {
  (void)hipFuncSetAttribute((const void*)gemm_kernel<MODE>, hipFuncAttributeMaxDynamicSharedMemorySize, Cfg<MODE>::LDS);
  gemm_kernel<MODE><<<256, 512 + 64 * NLD, Cfg<MODE>::LDS, stream>>>(ga);
}

extern "C" void kernel_launch(void* const* d_in, const int* in_sizes, int n_in, void* d_out, int out_size, void* d_ws,
                              size_t ws_size, hipStream_t stream) {
  const u16* x = (const u16*)d_in[0];
  const u16* wqkv = (const u16*)d_in[1];
  const u16* wo = (const u16*)d_in[2];
  const u16* w1 = (const u16*)d_in[4];
  const u16* w2 = (const u16*)d_in[5];
  const u16* w3 = (const u16*)d_in[6];
  const u16* w4 = (const u16*)d_in[7];
  char* ws = (char*)d_ws;
  u16 *Q = (u16*)(ws + WS_Q), *Kf = (u16*)(ws + WS_K), *Vf = (u16*)(ws + WS_V), *O = (u16*)(ws + WS_O),
      *H = (u16*)(ws + WS_H), *T = (u16*)(ws + WS_T), *X1 = (u16*)(ws + WS_X1), *U = (u16*)(ws + WS_U);
  float *PH = (float*)(ws + WS_PH), *PX = (float*)(ws + WS_PX);
  u16* out = (u16*)d_out;

  launch_gemm<1>(GArgs{x, wqkv, nullptr, nullptr, nullptr, nullptr, Q, Kf, Vf}, stream);
  (void)hipFuncSetAttribute((const void*)attn_kernel, hipFuncAttributeMaxDynamicSharedMemorySize, AT_LDS);
  attn_kernel<<<256, 640, AT_LDS, stream>>>(Q, Kf, Vf, O, wo, w1, w2, w3, w4);
  launch_gemm<3>(GArgs{O, wo, x, nullptr, nullptr, PH, H, nullptr, nullptr}, stream);
  launch_gemm<4>(GArgs{H, w1, nullptr, nullptr, PH, nullptr, T, nullptr, nullptr}, stream);
  launch_gemm<5>(GArgs{T, w2, H, nullptr, PH, PX, X1, nullptr, nullptr}, stream);
  launch_gemm<6>(GArgs{X1, w3, nullptr, nullptr, PX, nullptr, U, nullptr, nullptr}, stream);
  launch_gemm<7>(GArgs{U, w4, X1, H, nullptr, nullptr, out, nullptr, nullptr}, stream);
}
```

```cpp
#include <hip/hip_runtime.h>
#include <hip/hip_bf16.h>
#include <stdint.h>

typedef unsigned short u16;
typedef __attribute__((ext_vector_type(8))) short bf16x8;
typedef __attribute__((ext_vector_type(4))) short s16x4;
typedef __attribute__((ext_vector_type(2))) float f32x2;
typedef __attribute__((ext_vector_type(4))) float f32x4;
typedef __attribute__((ext_vector_type(16))) float f32x16;
typedef __attribute__((ext_vector_type(2))) __bf16 bf16x2_t;
typedef __attribute__((ext_vector_type(4))) unsigned u32x4;
typedef __attribute__((ext_vector_type(2))) unsigned u32x2;
typedef short v4i16_t __attribute__((ext_vector_type(4)));
typedef __attribute__((address_space(3))) const char* lds_cptr;

#define DEV __device__ __forceinline__
#define MFMA16(a, b, c) __builtin_amdgcn_mfma_f32_16x16x32_bf16((a), (b), (c), 0, 0, 0)
#define MFMA32(a, b, c) __builtin_amdgcn_mfma_f32_32x32x16_bf16((a), (b), (c), 0, 0, 0)

constexpr int SEQ = 2048, DM = 512, NH = 32, FF = 1376;

constexpr size_t WS_Q = 0, WS_K = 2u << 20, WS_V = 4u << 20, WS_O = 6u << 20, WS_H = 8u << 20, WS_PH = 10u << 20,
                 WS_PX = 11u << 20, WS_T = 12u << 20, WS_X1 = 14u << 20, WS_U = 16u << 20;

DEV unsigned cvtpk(float lo, float hi) {
  f32x2 v = {lo, hi};
  bf16x2_t b = __builtin_convertvector(v, bf16x2_t);
  return __builtin_bit_cast(unsigned, b);
}
DEV float lo2f(unsigned p) { return __uint_as_float(p << 16); }
DEV float hi2f(unsigned p) { return __uint_as_float(p & 0xffff0000u); }
DEV float rbf(float x) { return lo2f(cvtpk(x, x)); }

DEV void glds16(const void* gbase, unsigned voff, unsigned lds_base) {
  unsigned sv;
  asm volatile("s_mov_b32 %0, m0\n\ts_mov_b32 m0, %3\n\ts_nop 0\n\tglobal_load_lds_dwordx4 %1, %2\n\ts_mov_b32 m0, %0"
               : "=&s"(sv)
               : "v"(voff), "s"(gbase), "s"(lds_base)
               : "memory");
}
DEV u32x4 aload4(const void* gbase, unsigned voff) {
  u32x4 r;
  asm volatile("global_load_dwordx4 %0, %1, %2" : "=v"(r) : "v"(voff), "s"(gbase) : "memory");
  return r;
}
DEV u32x2 aload2(const void* gbase, unsigned voff) {
  u32x2 r;
  asm volatile("global_load_dwordx2 %0, %1, %2" : "=v"(r) : "v"(voff), "s"(gbase) : "memory");
  return r;
}
DEV void st_wt4(void* p, u32x4 v) { asm volatile("global_store_dwordx4 %0, %1, off sc1\n\ts_nop 1" ::"v"(p), "v"(v) : "memory"); }
DEV void st_wt2(void* p, u32x2 v) { asm volatile("global_store_dwordx2 %0, %1, off sc1\n\ts_nop 1" ::"v"(p), "v"(v) : "memory"); }
template <class T>
DEV void pin(T& x) { asm volatile("" : "+v"(x)); }
template <int N>
DEV void wait_vm_bar() {
  asm volatile("s_waitcnt vmcnt(%0) lgkmcnt(0)\n\ts_barrier" ::"i"(N) : "memory");
}
DEV void lds_bar() { asm volatile("s_waitcnt lgkmcnt(0)\n\ts_barrier" ::: "memory"); }
DEV bf16x8 lds_rd128(lds_cptr p) { return *(const __attribute__((address_space(3))) bf16x8*)p; }
DEV s16x4 lds_tr(lds_cptr p) {
  return __builtin_bit_cast(s16x4, __builtin_amdgcn_ds_read_tr16_b64_v4i16((__attribute__((address_space(3))) v4i16_t*)p));
}
template <int CTRL>
DEV float dpp_add(float v) {
  int x = __builtin_amdgcn_update_dpp(0, __float_as_int(v), CTRL, 0xf, 0xf, true);
  return v + __int_as_float(x);
}
DEV float add_x16(float v) {
  auto p = __builtin_amdgcn_permlane16_swap(__float_as_uint(v), __float_as_uint(v), false, false);
  return __uint_as_float(p[0]) + __uint_as_float(p[1]);
}
DEV float add_x32(float v) {
  auto p = __builtin_amdgcn_permlane32_swap(__float_as_uint(v), __float_as_uint(v), false, false);
  return __uint_as_float(p[0]) + __uint_as_float(p[1]);
}
DEV float max_x32(float v) {
  auto p = __builtin_amdgcn_permlane32_swap(__float_as_uint(v), __float_as_uint(v), false, false);
  return fmaxf(__uint_as_float(p[0]), __uint_as_float(p[1]));
}
DEV float wave_sum(float v) {
  v = dpp_add<0x121>(v);
  v = dpp_add<0x122>(v);
  v = dpp_add<0x124>(v);
  v = dpp_add<0x128>(v);
  return add_x32(add_x16(v));
}
DEV float dot2_ones(unsigned p, float acc) {
  return __builtin_amdgcn_fdot2_f32_bf16(__builtin_bit_cast(bf16x2_t, p), __builtin_bit_cast(bf16x2_t, 0x3F803F80u), acc, false);
}
DEV float dot2u(unsigned a, unsigned b, float acc) {
  return __builtin_amdgcn_fdot2_f32_bf16(__builtin_bit_cast(bf16x2_t, a), __builtin_bit_cast(bf16x2_t, b), acc, false);
}
DEV float gelu_tanh(float x) {
  const float c = -2.0f * 0.7978845608028654f * 1.4426950408889634f;
  float u = x * (1.0f + 0.044715f * x * x);
  float e = __builtin_amdgcn_exp2f(c * u);
  return x * __builtin_amdgcn_rcpf(1.0f + e);
}
DEV int bswz(int k) { return ((k >> 1) & 1) | (((k >> 3) & 1) << 1); }

#define STAMP(kid, i) do { } while (0)
#define DBG(kid, w, i) do { } while (0)

template <int I, int N, class F>
DEV void static_for(F&& f) {
  if constexpr (I < N) {
    f(std::integral_constant<int, I>{});
    static_for<I + 1, N>(f);
  }
}

DEV void ln_consts(float s, float q, float& mb, float& sd, float& m, float& v) {
  m = s * (1.0f / DM);
  v = fmaxf(q * (1.0f / DM) - m * m, 0.f);
  mb = rbf(m);
  sd = rbf(__builtin_sqrtf(rbf(rbf(v) + 1e-5f)));
}

constexpr int NLD = 4;
template <int MODE>
struct Cfg {
  static constexpr int NT = (MODE == 1 || MODE == 6) ? 3 : 1;
  static constexpr bool LN = (MODE == 1 || MODE == 4 || MODE == 6);
  static constexpr bool PST = (MODE == 4 || MODE == 5 || MODE == 6);
  static constexpr int K = (MODE == 7) ? FF : DM;
  static constexpr int LDA = K;
  static constexpr int LDW = (MODE == 1) ? 3 * DM : (MODE == 6 ? FF : DM);
  static constexpr int NSTAGE = (K + 63) / 64;
  static constexpr int SPB = (NT == 1) ? 2 : 1;
  static constexpr int NB = (NSTAGE + SPB - 1) / SPB;
  static constexpr int RS = 128 * NT;
  static constexpr int BBYTES = 64 * RS;
  static constexpr int SLOT = BBYTES + 8192;
  static constexpr int NGSLOT = 4;
  static constexpr int NSLOT = NGSLOT * SPB;
  static constexpr int DEPTH = NGSLOT - 1;
  static constexpr int X_ST = NSLOT * SLOT;
  static constexpr int X_CS = X_ST + 512;
  static constexpr int X_PX = X_CS + 1536;
  static constexpr int X_OUT = X_PX + 2048;
  static constexpr int RSO = 128 * NT + 16;
  static constexpr int LDS = X_OUT + (MODE == 1 ? 0 : 64 * RSO);
  static constexpr int LPS = 8 * (NT + 1) / NLD;
};

struct GArgs {
  const u16* A;
  const u16* W;
  const u16* r0;
  const u16* r1;
  const float* pin_;
  float* pout;
  u16* o0;
  u16* o1;
  u16* o2;
};

template <int MODE>
__global__ __launch_bounds__(512 + 64 * NLD, 2) void gemm_kernel(GArgs ga) {
  using C = Cfg<MODE>;
  constexpr int NT = C::NT;
  extern __shared__ __attribute__((aligned(16))) char lds[];
  STAMP(MODE, 0);
  const int tid = threadIdx.x, lane = tid & 63;
  const int wid = __builtin_amdgcn_readfirstlane(tid >> 6);
  const int xcd = blockIdx.x & 7, idx = blockIdx.x >> 3;
  const int rb = xcd * 4 + (idx >> 3), cb = idx & 7;
  const int row0 = rb * 64;
  const unsigned lds0 = (unsigned)(uintptr_t)lds;

  if (wid >= 8) {
    const int L = wid - 8;
    constexpr int IA = 8 / NLD, IB = 8 * NT / NLD;
    unsigned boff[IB];
    int bk[IB];
#pragma unroll
    for (int j = 0; j < IB; ++j) {
      int o = (L * IB + j) * 1024 + lane * 16;
      int k = o / C::RS, rem = o % C::RS;
      int grp = rem >> 7, blk = (rem >> 5) & 3, hf = (rem >> 4) & 1;
      int col;
      if (MODE == 1) col = grp * 512 + cb * 64;
      else col = cb * (64 * NT) + grp * 64;
      col += ((blk ^ bswz(k)) * 16) + hf * 8;
      if (MODE == 6 && col > FF - 8) col = FF - 8;
      bk[j] = k;
      boff[j] = (unsigned)(k * C::LDW + col) * 2u;
    }
    unsigned aoffj[IA];
#pragma unroll
    for (int j = 0; j < IA; ++j) {
      int r = (L * IA + j) * 8 + (lane >> 3), c = (lane & 7) ^ ((r >> 1) & 7);
      aoffj[j] = (unsigned)((row0 + r) * C::LDA + c * 8) * 2u;
    }
    auto issue_sub = [&](int s) {
      const unsigned slot = lds0 + (unsigned)((s % C::NSLOT) * C::SLOT);
#pragma unroll
      for (int j = 0; j < IA; ++j) glds16(ga.A, aoffj[j] + s * 128, slot + C::BBYTES + (L * IA + j) * 1024);
#pragma unroll
      for (int j = 0; j < IB; ++j) {
        unsigned off = boff[j] + (unsigned)(s * 64 * C::LDW) * 2u;
        if (MODE == 7 && s == C::NSTAGE - 1 && s * 64 + bk[j] >= FF) off = boff[j] + (unsigned)((FF - 64) * C::LDW) * 2u;
        glds16(ga.W, off, slot + (unsigned)(L * IB + j) * 1024u);
      }
    };
    auto issue_group = [&](auto g_) {
      constexpr int gg = g_.value;
      static_for<0, C::SPB>([&](auto u_) { if constexpr (gg < C::NB && gg * C::SPB + u_.value < C::NSTAGE) issue_sub(gg * C::SPB + u_.value); });
    };
    issue_group(std::integral_constant<int, 0>{});
    issue_group(std::integral_constant<int, 1>{});
    static_for<0, C::NB>([&](auto b_) {
      constexpr int b = b_.value;
      constexpr int lastg0 = (b == 0) ? 1 : b + C::DEPTH - 1;
      constexpr int lastg = lastg0 < C::NB - 1 ? lastg0 : C::NB - 1;
      constexpr int sub_after = ((lastg + 1) * C::SPB > C::NSTAGE ? C::NSTAGE : (lastg + 1) * C::SPB) - ((b + 1) * C::SPB > C::NSTAGE ? C::NSTAGE : (b + 1) * C::SPB);
      asm volatile("s_waitcnt vmcnt(%0)" ::"i"(sub_after * C::LPS) : "memory");
      if (wid == 8) DBG(MODE, 1, b);
      wait_vm_bar<sub_after * C::LPS>();
      if constexpr (b == 0) {
        static_for<2, C::DEPTH + 1>([&](auto g_) { issue_group(g_); });
      } else {
        issue_group(std::integral_constant<int, b + C::DEPTH>{});
      }
    });
    return;
  }

  const int wm = wid & 1, wn = wid >> 1;
  const int l15 = lane & 15, g = lane >> 4;
  u32x4 ps[2];
  u32x2 res0[2], res1[2];
  unsigned tv = 0;
  if constexpr (MODE == 1) {
    const char* tp = (tid < 384) ? (const char*)ga.W + (size_t)(idx * 384 + tid) * 128
                                 : (const char*)ga.A + (size_t)xcd * 262144 + (size_t)(idx * 64 + ((tid - 384) & 63)) * 128;
    tv = *(const volatile unsigned*)tp;
  }
  if constexpr (C::PST) {
    const float* pp = ga.pin_ + (size_t)(row0 + (tid >> 3)) * 64 + (tid & 7) * 8;
    ps[0] = *(const u32x4*)pp;
    ps[1] = *(const u32x4*)(pp + 4);
  }
  if constexpr (MODE == 3 || MODE == 5 || MODE == 7) {
#pragma unroll
    for (int mt = 0; mt < 2; ++mt) {
      size_t o = (size_t)(row0 + wm * 32 + mt * 16 + l15) * DM + cb * 64 + wn * 16 + 4 * g;
      res0[mt] = *(const u32x2*)(ga.r0 + o);
      if constexpr (MODE == 7) res1[mt] = *(const u32x2*)(ga.r1 + o);
    }
  }

  int xoff[2][2];
#pragma unroll
  for (int mt = 0; mt < 2; ++mt)
#pragma unroll
    for (int kk = 0; kk < 2; ++kk) {
      int r = wm * 32 + mt * 16 + l15;
      xoff[mt][kk] = C::BBYTES + r * 128 + (((4 * kk + g) ^ ((r >> 1) & 7)) * 16);
    }
  int woff[2][2];
  {
    int q = l15 >> 2, p = l15 & 3;
#pragma unroll
    for (int kk = 0; kk < 2; ++kk)
#pragma unroll
      for (int t = 0; t < 2; ++t) {
        int k = 32 * kk + 8 * g + 4 * t + q;
        woff[kk][t] = k * C::RS + ((wn ^ bswz(k)) * 32) + p * 8;
      }
  }

  f32x4 acc[2][NT];
  f32x4 cs[NT];
  const bf16x8 ones8 = {0x3F80, 0x3F80, 0x3F80, 0x3F80, 0x3F80, 0x3F80, 0x3F80, 0x3F80};
#pragma unroll
  for (int nt = 0; nt < NT; ++nt) {
    cs[nt] = f32x4{0.f, 0.f, 0.f, 0.f};
#pragma unroll
    for (int mt = 0; mt < 2; ++mt) acc[mt][nt] = f32x4{0.f, 0.f, 0.f, 0.f};
  }
  f32x2* st_lds = (f32x2*)(lds + C::X_ST);

    {
      if constexpr (C::PST) {
        float s_ = (__uint_as_float(ps[0][0]) + __uint_as_float(ps[0][2])) + (__uint_as_float(ps[1][0]) + __uint_as_float(ps[1][2]));
        float q_ = (__uint_as_float(ps[0][1]) + __uint_as_float(ps[0][3])) + (__uint_as_float(ps[1][1]) + __uint_as_float(ps[1][3]));
        s_ = dpp_add<0xB1>(s_); q_ = dpp_add<0xB1>(q_);
        s_ = dpp_add<0x4E>(s_); q_ = dpp_add<0x4E>(q_);
        s_ = dpp_add<0x141>(s_); q_ = dpp_add<0x141>(q_);
        float mb, sd, m, v;
        ln_consts(s_, q_, mb, sd, m, v);
        float M = mb, R = 1.0f / sd;
        if constexpr (MODE == 4) {
          float m2 = (m - mb) * R, v2 = v * R * R;
          float m2b = rbf(m2);
          float sd2 = rbf(__builtin_sqrtf(rbf(rbf(v2) + 1e-5f)));
          M = mb + m2b * sd;
          R = R / sd2;
        }
        if ((tid & 7) == 0) st_lds[tid >> 3] = f32x2{M, R};
      }
    }
  f32x4 sx[2] = {f32x4{0.f, 0.f, 0.f, 0.f}, f32x4{0.f, 0.f, 0.f, 0.f}}, qx[2] = {sx[0], sx[0]};
  static_for<0, C::NB>([&](auto b_) {
    constexpr int s = b_.value;
    if (wid == 0) DBG(MODE, 2, s);
    lds_bar();
    if (wid == 0) DBG(MODE, 0, s);
    if constexpr (s == 0) STAMP(MODE, 1);
    if constexpr (MODE == 1 && s == 2) asm volatile("" ::"v"(tv));
    constexpr int S0 = s * C::SPB;
    constexpr int NSUB = ((S0 + C::SPB > C::NSTAGE) ? C::NSTAGE : S0 + C::SPB) - S0;
    constexpr int NKS = 2 * NSUB - ((MODE == 7 && S0 + NSUB == C::NSTAGE) ? 1 : 0);
    bf16x8 xf[NKS][2], wf[NKS][NT];
#pragma unroll
    for (int ks = 0; ks < NKS; ++ks) {
      const lds_cptr bs = (lds_cptr)lds + ((S0 + (ks >> 1)) % C::NSLOT) * C::SLOT;
      const int kk = ks & 1;
#pragma unroll
      for (int nt = 0; nt < NT; ++nt) {
        s16x4 w0 = lds_tr(bs + woff[kk][0] + nt * 128);
        s16x4 w1 = lds_tr(bs + woff[kk][1] + nt * 128);
        wf[ks][nt] = __builtin_shufflevector(w0, w1, 0, 1, 2, 3, 4, 5, 6, 7);
        if (nt == 0) {
#pragma unroll
          for (int mt = 0; mt < 2; ++mt) xf[ks][mt] = lds_rd128(bs + xoff[mt][kk]);
        }
      }
    }
#pragma unroll
    for (int ks = 0; ks < NKS; ++ks) {
#pragma unroll
      for (int nt = 0; nt < NT; ++nt) {
#pragma unroll
        for (int mt = 0; mt < 2; ++mt) {
          if (MODE == 1 && nt == 2) acc[mt][nt] = MFMA16(xf[ks][mt], wf[ks][nt], acc[mt][nt]);
          else acc[mt][nt] = MFMA16(wf[ks][nt], xf[ks][mt], acc[mt][nt]);
        }
      }
    }
    if constexpr (C::LN) {
      auto colsum = [&](auto par_) {
#pragma unroll
        for (int ks = par_.value; ks < NKS; ks += 2)
#pragma unroll
          for (int nt = 0; nt < NT; ++nt) {
            if (MODE == 1 && nt == 2) cs[nt] = MFMA16(ones8, wf[ks][nt], cs[nt]);
            else cs[nt] = MFMA16(wf[ks][nt], ones8, cs[nt]);
          }
      };
      if (wm == 0) colsum(std::integral_constant<int, 0>{});
      else colsum(std::integral_constant<int, 1>{});
    }
    if constexpr (MODE == 1) {
      auto stat = [&](const bf16x8 (&xx)[2]) {
#pragma unroll
        for (int mt = 0; mt < 2; ++mt) {
          sx[mt] = MFMA16(ones8, xx[mt], sx[mt]);
          qx[mt] = MFMA16(xx[mt], xx[mt], qx[mt]);
        }
      };
      if (wn == ((2 * s) & 3)) stat(xf[0]);
      else if (wn == ((2 * s + 1) & 3)) stat(xf[1]);
    }
    __builtin_amdgcn_sched_barrier(0);
  });

  float* cs_lds = (float*)(lds + C::X_CS);
  if constexpr (C::LN) {
#pragma unroll
    for (int nt = 0; nt < NT; ++nt) {
      float* d = cs_lds + ((wm * 4 + wn) * NT + nt) * 16;
      if (MODE == 1 && nt == 2) { if (g == 0) d[l15] = cs[nt][0]; }
      else if (l15 == 0) *(f32x4*)(d + 4 * g) = cs[nt];
    }
    if constexpr (MODE == 1) {
      f32x2* px = (f32x2*)(lds + C::X_PX);
      const int di = l15 & 3;
      float b0 = di == 0 ? qx[0][0] : di == 1 ? qx[0][1] : di == 2 ? qx[0][2] : qx[0][3];
      float b1 = di == 0 ? qx[1][0] : di == 1 ? qx[1][1] : di == 2 ? qx[1][2] : qx[1][3];
      if (g == (l15 >> 2)) { px[wn * 64 + wm * 32 + l15] = f32x2{sx[0][0], b0}; px[wn * 64 + wm * 32 + 16 + l15] = f32x2{sx[1][0], b1}; }
      lds_bar();
      if (tid < 64) {
        f32x2 t = (px[tid] + px[64 + tid]) + (px[128 + tid] + px[192 + tid]);
        float mb, sd, m, v;
        ln_consts(t[0], t[1], mb, sd, m, v);
        st_lds[tid] = f32x2{mb, 1.0f / sd};
      }
    }
    lds_bar();
#pragma unroll
    for (int nt = 0; nt < NT; ++nt) {
      const float* d = cs_lds + (((1 - wm) * 4 + wn) * NT + nt) * 16;
      if (MODE == 1 && nt == 2) cs[nt][0] += d[l15];
      else cs[nt] += *(const f32x4*)(d + 4 * g);
    }
  }
  STAMP(MODE, 2);
  char* ost = lds + C::X_OUT;
#pragma unroll
  for (int mt = 0; mt < 2; ++mt) {
    const int lrow = wm * 32 + mt * 16 + l15;
    const int tok = row0 + lrow;
#pragma unroll
    for (int nt = 0; nt < NT; ++nt) {
      f32x4 a = acc[mt][nt];
      if constexpr (C::LN) {
        if (MODE == 1 && nt == 2) {
          float c = cs[nt][0];
#pragma unroll
          for (int i = 0; i < 4; ++i) {
            f32x2 st = st_lds[wm * 32 + mt * 16 + 4 * g + i];
            a[i] = (a[i] - st[0] * c) * st[1];
          }
        } else {
          f32x4 c = cs[nt];
          f32x2 st = st_lds[lrow];
#pragma unroll
          for (int i = 0; i < 4; ++i) a[i] = (a[i] - st[0] * c[i]) * st[1];
        }
      }
      u32x2 v;
      if constexpr (MODE == 1) {
        const int head = cb * 4 + wn;
        if (nt < 2) {
          float sc = (nt == 0) ? 0.25f * 1.4426950408889634f : 1.0f;
          v = u32x2{cvtpk(a[0] * sc, a[1] * sc), cvtpk(a[2] * sc, a[3] * sc)};
          u16* dst = (nt == 0 ? ga.o0 : ga.o1) + ((size_t)(head * 64 + (tok >> 5)) * 2 + (g >> 1)) * 256 + (tok & 31) * 8 + (g & 1) * 4;
          st_wt2(dst, v);
        } else {
          const int step = (row0 + wm * 32 + mt * 16) >> 4;
          v = u32x2{cvtpk(a[0], a[1]), cvtpk(a[2], a[3])};
          u16* dst = ga.o2 + ((size_t)(head * 128 + step) * 2 + (g & 1)) * 128 + l15 * 8 + (g >> 1) * 4;
          st_wt2(dst, v);
        }
      } else if constexpr (MODE == 3 || MODE == 5) {
        u32x2 r = res0[mt];
        if constexpr (MODE == 5) {
          f32x2 st = st_lds[lrow];
          unsigned d0 = cvtpk(lo2f(r[0]) - st[0], hi2f(r[0]) - st[0]), d1 = cvtpk(lo2f(r[1]) - st[0], hi2f(r[1]) - st[0]);
          r[0] = cvtpk(lo2f(d0) * st[1], hi2f(d0) * st[1]);
          r[1] = cvtpk(lo2f(d1) * st[1], hi2f(d1) * st[1]);
        }
        unsigned a0 = cvtpk(a[0], a[1]), a1 = cvtpk(a[2], a[3]);
        v = u32x2{cvtpk(lo2f(r[0]) + lo2f(a0), hi2f(r[0]) + hi2f(a0)), cvtpk(lo2f(r[1]) + lo2f(a1), hi2f(r[1]) + hi2f(a1))};
        float f0 = lo2f(v[0]), f1 = hi2f(v[0]), f2 = lo2f(v[1]), f3 = hi2f(v[1]);
        float s_ = (f0 + f1) + (f2 + f3), q_ = fmaf(f0, f0, f1 * f1) + fmaf(f2, f2, f3 * f3);
        s_ = add_x32(add_x16(s_)); q_ = add_x32(add_x16(q_));
        if (g == 0) *(f32x2*)(ga.pout + (size_t)tok * 64 + (cb * 4 + wn) * 2) = f32x2{s_, q_};
      } else if constexpr (MODE == 4 || MODE == 6) {
        unsigned a0 = cvtpk(a[0], a[1]), a1 = cvtpk(a[2], a[3]);
        v = u32x2{cvtpk(gelu_tanh(lo2f(a0)), gelu_tanh(hi2f(a0))), cvtpk(gelu_tanh(lo2f(a1)), gelu_tanh(hi2f(a1)))};
      } else {
        u32x2 x1 = res0[mt], hh = res1[mt];
        unsigned a0 = cvtpk(a[0], a[1]), a1 = cvtpk(a[2], a[3]);
        unsigned b0 = cvtpk(lo2f(x1[0]) + lo2f(a0), hi2f(x1[0]) + hi2f(a0));
        unsigned b1 = cvtpk(lo2f(x1[1]) + lo2f(a1), hi2f(x1[1]) + hi2f(a1));
        v = u32x2{cvtpk(lo2f(hh[0]) + lo2f(b0), hi2f(hh[0]) + hi2f(b0)), cvtpk(lo2f(hh[1]) + lo2f(b1), hi2f(hh[1]) + hi2f(b1))};
      }
      if constexpr (MODE != 1) *(u32x2*)(ost + lrow * C::RSO + nt * 128 + wn * 32 + g * 8) = v;
    }
  }
  if constexpr (MODE != 1) {
    lds_bar();
#pragma unroll
    for (int j = 0; j < NT; ++j) {
      const int c = tid + 512 * j, row = c / (8 * NT), cc = c % (8 * NT);
      const u32x4 v = *(const u32x4*)(ost + row * C::RSO + cc * 16);
      const int tok = row0 + row;
      if constexpr (MODE == 4) {
        const int grp = cb >> 1;
        const int sh = (grp == 0) ? 1 : (grp == 1) ? -1 : (grp == 2) ? 2 : -2;
        const int col = cb * 64 + cc * 8, dt = tok + sh, src = tok - sh;
        if (dt >= 0 && dt < SEQ) st_wt4(ga.o0 + (size_t)dt * DM + col, v);
        if (src < 0 || src >= SEQ) *(u32x4*)(ga.o0 + (size_t)tok * DM + col) = u32x4{0u, 0u, 0u, 0u};
      } else if constexpr (MODE == 6) {
        const int col = cb * 192 + cc * 8;
        if (col < FF) st_wt4(ga.o0 + (size_t)tok * FF + col, v);
      } else {
        st_wt4(ga.o0 + (size_t)tok * DM + cb * 64 + cc * 8, v);
      }
    }
  }
  STAMP(MODE, 3);
}

constexpr int AT_K = 0, AT_V = 65536, AT_ONES = 131072, AT_Q = AT_ONES + 8192, AT_LDS = AT_Q + 8192;

__global__ __launch_bounds__(640, 2) void attn_kernel(const u16* Qf, const u16* Kf, const u16* Vf, u16* O, const u16* wa,
                                                       const u16* wb, const u16* wc, const u16* wd, const u16* we) {
  extern __shared__ __attribute__((aligned(16))) char lds[];
  STAMP(2, 0);
  const int tid = threadIdx.x, lane = tid & 63;
  const int wid = __builtin_amdgcn_readfirstlane(tid >> 6);
  const int xcd = blockIdx.x & 7, idx = blockIdx.x >> 3;
  const int head = xcd * 4 + (idx >> 3), qb = idx & 7;
  const unsigned lds0 = (unsigned)(uintptr_t)lds;
  const unsigned lo = lane * 16;

  if (wid >= 8) {
    const int L = wid - 8;
    const char* src = (const char*)(L ? Vf : Kf) + (size_t)head * 65536;
    const char* qg = (const char*)Qf + (size_t)head * 65536 + (qb * 8 + L * 4) * 1024;
    const unsigned dst = lds0 + (L ? AT_V : AT_K);
#pragma unroll
    for (int j = 0; j < 4; ++j) glds16(qg, lo + j * 1024, lds0 + AT_Q + (L * 4 + j) * 1024);
#pragma unroll
    for (int j = 0; j < 16; ++j) glds16(src, lo + j * 1024, dst + j * 1024);
    wait_vm_bar<0>();
#pragma unroll
    for (int j = 16; j < 64; ++j) glds16(src, lo + j * 1024, dst + j * 1024);
    unsigned tw[5][3];
#pragma unroll
    for (int j = 0; j < 3; ++j) {
      unsigned t = (unsigned)((idx * 2 + L) * 192 + j * 64 + lane) * 128u;
      unsigned ta = t < 512u * 1024u ? t : 0u, tb = t < (unsigned)(DM * FF * 2) ? t : 0u;
      asm volatile("global_load_dword %0, %5, %7\n\tglobal_load_dword %1, %5, %8\n\tglobal_load_dword %2, %5, %9\n\t"
                   "global_load_dword %3, %6, %10\n\tglobal_load_dword %4, %6, %11"
                   : "=&v"(tw[0][j]), "=&v"(tw[1][j]), "=&v"(tw[2][j]), "=&v"(tw[3][j]), "=&v"(tw[4][j])
                   : "v"(ta), "v"(tb), "s"(wa), "s"(wb), "s"(wc), "s"(wd), "s"(we)
                   : "memory");
    }
    asm volatile("s_waitcnt vmcnt(15)\n\ts_barrier\n\ts_waitcnt vmcnt(0)"
                 : "+v"(tw[0][0]), "+v"(tw[1][0]), "+v"(tw[2][0]), "+v"(tw[3][0]), "+v"(tw[4][0]), "+v"(tw[0][1]), "+v"(tw[1][1]),
                   "+v"(tw[2][1]), "+v"(tw[3][1]), "+v"(tw[4][1]), "+v"(tw[0][2]), "+v"(tw[1][2]), "+v"(tw[2][2]), "+v"(tw[3][2]),
                   "+v"(tw[4][2])::"memory");
    return;
  }
  const int r = lane & 31, h = lane >> 5;
  const int qt = wid;
  *(u32x4*)(lds + AT_ONES + tid * 16) = u32x4{0x3F803F80u, 0x3F803F80u, 0x3F803F80u, 0x3F803F80u};

  lds_cptr kp = (lds_cptr)lds + AT_K + lane * 16;
  lds_cptr vp = (lds_cptr)lds + ((r < 16) ? (AT_V + h * 256 + r * 16) : (AT_ONES + (r - 16) * 16));
  const int vstep = (r < 16) ? 4096 : 0;

  f32x16 o, negm, sA, sB;
#pragma unroll
  for (int i = 0; i < 16; ++i) { o[i] = 0.f; negm[i] = 0.f; }

  lds_bar();
  STAMP(2, 1);
  const bf16x8 qf = lds_rd128((lds_cptr)lds + AT_Q + qt * 1024 + lane * 16);
  bf16x8 kfA, kfB, vA0, vA1, vB0, vB1;
  u32x4 pA0, pA1, pB0, pB1;
  {
    sA = MFMA32(lds_rd128(kp), qf, negm);
    kfA = lds_rd128(kp + 1024);
    float mx = sA[0];
#pragma unroll
    for (int i = 1; i < 16; ++i) mx = fmaxf(mx, sA[i]);
    const float d = max_x32(mx) + 7.0f;
#pragma unroll
    for (int i = 0; i < 16; ++i) { negm[i] = -d; sA[i] -= d; }
    vB0 = lds_rd128(vp); vB1 = vB0;
    pB0 = u32x4{0u, 0u, 0u, 0u}; pB1 = pB0;
  }

#define SBAR() __builtin_amdgcn_sched_barrier(0)
#define PK(x, i) __builtin_amdgcn_perm(__float_as_uint(x[(i) + 1]), __float_as_uint(x[i]), 0x07060302u)
#define EXPS(lo_, hi_) _Pragma("unroll") for (int i = lo_; i < hi_; ++i) sc[i] = __builtin_amdgcn_exp2f(sc[i])
  auto step = [&](f32x16& sc, f32x16& sn, const bf16x8& kf, bf16x8& kfn, const bf16x8& vp0, const bf16x8& vp1,
                  const u32x4& pp0, const u32x4& pp1, bf16x8& vc0, bf16x8& vc1, u32x4& pc0, u32x4& pc1, lds_cptr kc,
                  lds_cptr vc) {
    sn = MFMA32(kf, qf, negm);
    kfn = lds_rd128(kc + 2048);
    vc0 = lds_rd128(vc); vc1 = lds_rd128(vc + 512);
    SBAR();
    EXPS(0, 5);
    SBAR();
    o = MFMA32(vp0, __builtin_bit_cast(bf16x8, pp0), o);
    SBAR();
    EXPS(5, 11);
    SBAR();
    o = MFMA32(vp1, __builtin_bit_cast(bf16x8, pp1), o);
    SBAR();
    EXPS(11, 16);
    SBAR();
#pragma unroll
    for (int i = 0; i < 4; ++i) { pc0[i] = PK(sc, 2 * i); pc1[i] = PK(sc, 8 + 2 * i); }
    unsigned chk = (pc0[0] | pc0[1] | pc0[2]) | (pc0[3] | pc1[0] | pc1[1]) | (pc1[2] | pc1[3]);
    if (__builtin_expect(__any((chk & 0x40004000u) != 0), 0)) {
      f32x16 s2 = MFMA32(lds_rd128(kc), qf, negm);
      float mx = s2[0];
#pragma unroll
      for (int i = 1; i < 16; ++i) mx = fmaxf(mx, s2[i]);
      const float d = fmaxf(max_x32(mx) + 7.0f, 0.f);
      const float alpha = __builtin_amdgcn_exp2f(-d);
#pragma unroll
      for (int i = 0; i < 16; ++i) { o[i] *= alpha; negm[i] -= d; sn[i] -= d; s2[i] = __builtin_amdgcn_exp2f(s2[i] - d); }
#pragma unroll
      for (int i = 0; i < 4; ++i) { pc0[i] = PK(s2, 2 * i); pc1[i] = PK(s2, 8 + 2 * i); }
    }
    SBAR();
  };
  auto group = [&]() {
    step(sA, sB, kfA, kfB, vB0, vB1, pB0, pB1, vA0, vA1, pA0, pA1, kp, vp);
    step(sB, sA, kfB, kfA, vA0, vA1, pA0, pA1, vB0, vB1, pB0, pB1, kp + 1024, vp + 1024);
    step(sA, sB, kfA, kfB, vB0, vB1, pB0, pB1, vA0, vA1, pA0, pA1, kp + 2048, vp + 2048);
    step(sB, sA, kfB, kfA, vA0, vA1, pA0, pA1, vB0, vB1, pB0, pB1, kp + 3072, vp + 3072);
    kp += 4096;
    vp += vstep;
  };
#pragma unroll 1
  for (int it = 0; it < 3; ++it) group();
  lds_bar();
#pragma unroll 1
  for (int it = 3; it < 16; ++it) group();
  o = MFMA32(vB0, __builtin_bit_cast(bf16x8, pB0), o);
  o = MFMA32(vB1, __builtin_bit_cast(bf16x8, pB1), o);

  STAMP(2, 2);
  {
    const float inv = 1.0f / o[8];
    const int q = qb * 256 + qt * 32 + r;
    u16* dst = O + (size_t)q * DM + head * 16 + 4 * h;
    *(u32x2*)dst = u32x2{cvtpk(o[0] * inv, o[1] * inv), cvtpk(o[2] * inv, o[3] * inv)};
    *(u32x2*)(dst + 8) = u32x2{cvtpk(o[4] * inv, o[5] * inv), cvtpk(o[6] * inv, o[7] * inv)};
  }
  STAMP(2, 3);
}

template <int MODE>
static void launch_gemm(const GArgs& ga, hipStream_t stream) {
  (void)hipFuncSetAttribute((const void*)gemm_kernel<MODE>, hipFuncAttributeMaxDynamicSharedMemorySize, Cfg<MODE>::LDS);
  gemm_kernel<MODE><<<256, 512 + 64 * NLD, Cfg<MODE>::LDS, stream>>>(ga);
}

extern "C" void kernel_launch(void* const* d_in, const int* in_sizes, int n_in, void* d_out, int out_size, void* d_ws,
                              size_t ws_size, hipStream_t stream) {
  const u16* x = (const u16*)d_in[0];
  const u16* wqkv = (const u16*)d_in[1];
  const u16* wo = (const u16*)d_in[2];
  const u16* w1 = (const u16*)d_in[4];
  const u16* w2 = (const u16*)d_in[5];
  const u16* w3 = (const u16*)d_in[6];
  const u16* w4 = (const u16*)d_in[7];
  char* ws = (char*)d_ws;
  u16 *Q = (u16*)(ws + WS_Q), *Kf = (u16*)(ws + WS_K), *Vf = (u16*)(ws + WS_V), *O = (u16*)(ws + WS_O),
      *H = (u16*)(ws + WS_H), *T = (u16*)(ws + WS_T), *X1 = (u16*)(ws + WS_X1), *U = (u16*)(ws + WS_U);
  float *PH = (float*)(ws + WS_PH), *PX = (float*)(ws + WS_PX);
  u16* out = (u16*)d_out;

  launch_gemm<1>(GArgs{x, wqkv, nullptr, nullptr, nullptr, nullptr, Q, Kf, Vf}, stream);
  (void)hipFuncSetAttribute((const void*)attn_kernel, hipFuncAttributeMaxDynamicSharedMemorySize, AT_LDS);
  attn_kernel<<<256, 640, AT_LDS, stream>>>(Q, Kf, Vf, O, wo, w1, w2, w3, w4);
  launch_gemm<3>(GArgs{O, wo, x, nullptr, nullptr, PH, H, nullptr, nullptr}, stream);
  launch_gemm<4>(GArgs{H, w1, nullptr, nullptr, PH, nullptr, T, nullptr, nullptr}, stream);
  launch_gemm<5>(GArgs{T, w2, H, nullptr, PH, PX, X1, nullptr, nullptr}, stream);
  launch_gemm<6>(GArgs{X1, w3, nullptr, nullptr, PX, nullptr, U, nullptr, nullptr}, stream);
  launch_gemm<7>(GArgs{U, w4, X1, H, nullptr, nullptr, out, nullptr, nullptr}, stream);
}
```

```cpp
#include <hip/hip_runtime.h>
#include <hip/hip_bf16.h>
#include <stdint.h>

typedef unsigned short u16;
typedef __attribute__((ext_vector_type(8))) short bf16x8;
typedef __attribute__((ext_vector_type(4))) short s16x4;
typedef __attribute__((ext_vector_type(2))) float f32x2;
typedef __attribute__((ext_vector_type(4))) float f32x4;
typedef __attribute__((ext_vector_type(16))) float f32x16;
typedef __attribute__((ext_vector_type(2))) __bf16 bf16x2_t;
typedef __attribute__((ext_vector_type(4))) unsigned u32x4;
typedef __attribute__((ext_vector_type(2))) unsigned u32x2;
typedef short v4i16_t __attribute__((ext_vector_type(4)));
typedef __attribute__((address_space(3))) const char* lds_cptr;

#define DEV __device__ __forceinline__
#define MFMA16(a, b, c) __builtin_amdgcn_mfma_f32_16x16x32_bf16((a), (b), (c), 0, 0, 0)
#define MFMA32(a, b, c) __builtin_amdgcn_mfma_f32_32x32x16_bf16((a), (b), (c), 0, 0, 0)

constexpr int SEQ = 2048, DM = 512, NH = 32, FF = 1376;

constexpr size_t WS_Q = 0, WS_K = 2u << 20, WS_V = 4u << 20, WS_O = 6u << 20, WS_H = 8u << 20, WS_PH = 10u << 20,
                 WS_PX = 11u << 20, WS_KN = (11u << 20) + (768u << 10), WS_T = 12u << 20, WS_X1 = 14u << 20, WS_U = 16u << 20;

DEV unsigned cvtpk(float lo, float hi) {
  f32x2 v = {lo, hi};
  bf16x2_t b = __builtin_convertvector(v, bf16x2_t);
  return __builtin_bit_cast(unsigned, b);
}
DEV float lo2f(unsigned p) { return __uint_as_float(p << 16); }
DEV float hi2f(unsigned p) { return __uint_as_float(p & 0xffff0000u); }
DEV float rbf(float x) { return lo2f(cvtpk(x, x)); }

DEV void glds16(const void* gbase, unsigned voff, unsigned lds_base) {
  unsigned sv;
  asm volatile("s_mov_b32 %0, m0\n\ts_mov_b32 m0, %3\n\ts_nop 0\n\tglobal_load_lds_dwordx4 %1, %2\n\ts_mov_b32 m0, %0"
               : "=&s"(sv)
               : "v"(voff), "s"(gbase), "s"(lds_base)
               : "memory");
}
DEV void st_wt4(void* p, u32x4 v) { asm volatile("global_store_dwordx4 %0, %1, off sc1\n\ts_nop 1" ::"v"(p), "v"(v) : "memory"); }
DEV void st_wt2(void* p, u32x2 v) { asm volatile("global_store_dwordx2 %0, %1, off sc1\n\ts_nop 1" ::"v"(p), "v"(v) : "memory"); }
template <class T>
DEV void pin(T& x) { asm volatile("" : "+v"(x)); }
template <int N>
DEV void wait_vm_bar() {
  asm volatile("s_waitcnt vmcnt(%0) lgkmcnt(0)\n\ts_barrier" ::"i"(N) : "memory");
}
DEV void lds_bar() { asm volatile("s_waitcnt lgkmcnt(0)\n\ts_barrier" ::: "memory"); }
DEV bf16x8 lds_rd128(lds_cptr p) { return *(const __attribute__((address_space(3))) bf16x8*)p; }
DEV s16x4 lds_tr(lds_cptr p) {
  return __builtin_bit_cast(s16x4, __builtin_amdgcn_ds_read_tr16_b64_v4i16((__attribute__((address_space(3))) v4i16_t*)p));
}
template <int CTRL>
DEV float dpp_add(float v) {
  int x = __builtin_amdgcn_update_dpp(0, __float_as_int(v), CTRL, 0xf, 0xf, true);
  return v + __int_as_float(x);
}
DEV float add_x16(float v) {
  auto p = __builtin_amdgcn_permlane16_swap(__float_as_uint(v), __float_as_uint(v), false, false);
  return __uint_as_float(p[0]) + __uint_as_float(p[1]);
}
DEV float add_x32(float v) {
  auto p = __builtin_amdgcn_permlane32_swap(__float_as_uint(v), __float_as_uint(v), false, false);
  return __uint_as_float(p[0]) + __uint_as_float(p[1]);
}
DEV float max_x32(float v) {
  auto p = __builtin_amdgcn_permlane32_swap(__float_as_uint(v), __float_as_uint(v), false, false);
  return fmaxf(__uint_as_float(p[0]), __uint_as_float(p[1]));
}
template <int CTRL>
DEV float dpp_max(float v) {
  int x = __builtin_amdgcn_update_dpp(0, __float_as_int(v), CTRL, 0xf, 0xf, true);
  return fmaxf(v, __int_as_float(x));
}
DEV float row16_max(float v) { return dpp_max<0x128>(dpp_max<0x124>(dpp_max<0x122>(dpp_max<0x121>(v)))); }
DEV float max_x16(float v) {
  auto p = __builtin_amdgcn_permlane16_swap(__float_as_uint(v), __float_as_uint(v), false, false);
  return fmaxf(__uint_as_float(p[0]), __uint_as_float(p[1]));
}
DEV float wave_sum(float v) {
  v = dpp_add<0x121>(v);
  v = dpp_add<0x122>(v);
  v = dpp_add<0x124>(v);
  v = dpp_add<0x128>(v);
  return add_x32(add_x16(v));
}
DEV float dot2_ones(unsigned p, float acc) {
  return __builtin_amdgcn_fdot2_f32_bf16(__builtin_bit_cast(bf16x2_t, p), __builtin_bit_cast(bf16x2_t, 0x3F803F80u), acc, false);
}
DEV float dot2u(unsigned a, unsigned b, float acc) {
  return __builtin_amdgcn_fdot2_f32_bf16(__builtin_bit_cast(bf16x2_t, a), __builtin_bit_cast(bf16x2_t, b), acc, false);
}
DEV float gelu_tanh(float x) {
  const float c = -2.0f * 0.7978845608028654f * 1.4426950408889634f;
  float u = x * (1.0f + 0.044715f * x * x);
  float e = __builtin_amdgcn_exp2f(c * u);
  return x * __builtin_amdgcn_rcpf(1.0f + e);
}
DEV int bswz(int k) { return ((k >> 1) & 1) | (((k >> 3) & 1) << 1); }

template <int I, int N, class F>
DEV void static_for(F&& f) {
  if constexpr (I < N) {
    f(std::integral_constant<int, I>{});
    static_for<I + 1, N>(f);
  }
}

DEV void ln_consts(float s, float q, float& mb, float& sd, float& m, float& v) {
  m = s * (1.0f / DM);
  v = fmaxf(q * (1.0f / DM) - m * m, 0.f);
  mb = rbf(m);
  sd = rbf(__builtin_sqrtf(rbf(rbf(v) + 1e-5f)));
}

constexpr int NLD = 4;
template <int MODE>
struct Cfg {
  static constexpr int NT = (MODE == 1 || MODE == 6) ? 3 : 1;
  static constexpr bool LN = (MODE == 1 || MODE == 4 || MODE == 6);
  static constexpr bool PST = (MODE == 4 || MODE == 5 || MODE == 6);
  static constexpr int K = (MODE == 7) ? FF : DM;
  static constexpr int LDA = K;
  static constexpr int LDW = (MODE == 1) ? 3 * DM : (MODE == 6 ? FF : DM);
  static constexpr int NSTAGE = (K + 63) / 64;
  static constexpr int SPB = (NT == 1) ? 2 : 1;
  static constexpr int NB = (NSTAGE + SPB - 1) / SPB;
  static constexpr int RS = 128 * NT;
  static constexpr int BBYTES = 64 * RS;
  static constexpr int SLOT = BBYTES + 8192;
  static constexpr int NGSLOT = 4;
  static constexpr int NSLOT = NGSLOT * SPB;
  static constexpr int DEPTH = NGSLOT - 1;
  static constexpr int X_ST = NSLOT * SLOT;
  static constexpr int X_CS = X_ST + 512;
  static constexpr int X_PX = X_CS + 1536;
  static constexpr int X_OUT = X_PX + 2048;
  static constexpr int RSO = 128 * NT + 16;
  static constexpr int LDS = X_OUT + (MODE == 1 ? 0 : 64 * RSO);
  static constexpr int LPS = 8 * (NT + 1) / NLD;
};

struct GArgs {
  const u16* A;
  const u16* W;
  const u16* r0;
  const u16* r1;
  const float* pin_;
  float* pout;
  u16* o0;
  u16* o1;
  u16* o2;
};

template <int MODE>
__global__ __launch_bounds__(512 + 64 * NLD, 2) void gemm_kernel(GArgs ga) {
  using C = Cfg<MODE>;
  constexpr int NT = C::NT;
  extern __shared__ __attribute__((aligned(16))) char lds[];
  const int tid = threadIdx.x, lane = tid & 63;
  const int wid = __builtin_amdgcn_readfirstlane(tid >> 6);
  const int xcd = blockIdx.x & 7, idx = blockIdx.x >> 3;
  const int rb = xcd * 4 + (idx >> 3), cb = idx & 7;
  const int row0 = rb * 64;
  const unsigned lds0 = (unsigned)(uintptr_t)lds;

  if (wid >= 8) {
    const int L = wid - 8;
    constexpr int IA = 8 / NLD, IB = 8 * NT / NLD;
    unsigned boff[IB];
    int bk[IB];
#pragma unroll
    for (int j = 0; j < IB; ++j) {
      int o = (L * IB + j) * 1024 + lane * 16;
      int k = o / C::RS, rem = o % C::RS;
      int grp = rem >> 7, blk = (rem >> 5) & 3, hf = (rem >> 4) & 1;
      int col;
      if (MODE == 1) col = grp * 512 + cb * 64;
      else col = cb * (64 * NT) + grp * 64;
      col += ((blk ^ bswz(k)) * 16) + hf * 8;
      if (MODE == 6 && col > FF - 8) col = FF - 8;
      bk[j] = k;
      boff[j] = (unsigned)(k * C::LDW + col) * 2u;
    }
    unsigned aoffj[IA];
#pragma unroll
    for (int j = 0; j < IA; ++j) {
      int r = (L * IA + j) * 8 + (lane >> 3), c = (lane & 7) ^ ((r >> 1) & 7);
      aoffj[j] = (unsigned)((row0 + r) * C::LDA + c * 8) * 2u;
    }
    auto issue_sub = [&](int s) {
      const unsigned slot = lds0 + (unsigned)((s % C::NSLOT) * C::SLOT);
#pragma unroll
      for (int j = 0; j < IA; ++j) glds16(ga.A, aoffj[j] + s * 128, slot + C::BBYTES + (L * IA + j) * 1024);
#pragma unroll
      for (int j = 0; j < IB; ++j) {
        unsigned off = boff[j] + (unsigned)(s * 64 * C::LDW) * 2u;
        if (MODE == 7 && s == C::NSTAGE - 1 && s * 64 + bk[j] >= FF) off = boff[j] + (unsigned)((FF - 64) * C::LDW) * 2u;
        glds16(ga.W, off, slot + (unsigned)(L * IB + j) * 1024u);
      }
    };
    auto issue_group = [&](auto g_) {
      constexpr int gg = g_.value;
      static_for<0, C::SPB>([&](auto u_) { if constexpr (gg < C::NB && gg * C::SPB + u_.value < C::NSTAGE) issue_sub(gg * C::SPB + u_.value); });
    };
    issue_group(std::integral_constant<int, 0>{});
    issue_group(std::integral_constant<int, 1>{});
    static_for<0, C::NB>([&](auto b_) {
      constexpr int b = b_.value;
      constexpr int Tp = (b == 0) ? 1 : ((b - 1 + C::DEPTH < 2 * (b - 1) + 3) ? b - 1 + C::DEPTH : 2 * (b - 1) + 3);
      constexpr int Tn = (b + C::DEPTH < 2 * b + 3) ? b + C::DEPTH : 2 * b + 3;
      constexpr int lastg = Tp < C::NB - 1 ? Tp : C::NB - 1;
      constexpr int sub_after = ((lastg + 1) * C::SPB > C::NSTAGE ? C::NSTAGE : (lastg + 1) * C::SPB) - ((b + 1) * C::SPB > C::NSTAGE ? C::NSTAGE : (b + 1) * C::SPB);
      wait_vm_bar<sub_after * C::LPS>();
      static_for<Tp + 1, Tn + 1>([&](auto g_) { issue_group(g_); });
    });
    return;
  }

  const int wm = wid & 1, wn = wid >> 1;
  const int l15 = lane & 15, g = lane >> 4;
  u32x4 ps[2];
  u32x2 res0[2], res1[2];
  if constexpr (C::PST) {
    const float* pp = ga.pin_ + (size_t)(row0 + (tid >> 3)) * 64 + (tid & 7) * 8;
    ps[0] = *(const u32x4*)pp;
    ps[1] = *(const u32x4*)(pp + 4);
  }
  if constexpr (MODE == 3 || MODE == 5 || MODE == 7) {
#pragma unroll
    for (int mt = 0; mt < 2; ++mt) {
      size_t o = (size_t)(row0 + wm * 32 + mt * 16 + l15) * DM + cb * 64 + wn * 16 + 4 * g;
      res0[mt] = *(const u32x2*)(ga.r0 + o);
      if constexpr (MODE == 7) res1[mt] = *(const u32x2*)(ga.r1 + o);
    }
  }

  int xoff[2][2];
#pragma unroll
  for (int mt = 0; mt < 2; ++mt)
#pragma unroll
    for (int kk = 0; kk < 2; ++kk) {
      int r = wm * 32 + mt * 16 + l15;
      xoff[mt][kk] = C::BBYTES + r * 128 + (((4 * kk + g) ^ ((r >> 1) & 7)) * 16);
    }
  int woff[2][2];
  {
    int q = l15 >> 2, p = l15 & 3;
#pragma unroll
    for (int kk = 0; kk < 2; ++kk)
#pragma unroll
      for (int t = 0; t < 2; ++t) {
        int k = 32 * kk + 8 * g + 4 * t + q;
        woff[kk][t] = k * C::RS + ((wn ^ bswz(k)) * 32) + p * 8;
      }
  }

  f32x4 acc[2][NT];
  f32x4 cs[NT];
  const bf16x8 ones8 = {0x3F80, 0x3F80, 0x3F80, 0x3F80, 0x3F80, 0x3F80, 0x3F80, 0x3F80};
#pragma unroll
  for (int nt = 0; nt < NT; ++nt) {
    cs[nt] = f32x4{0.f, 0.f, 0.f, 0.f};
#pragma unroll
    for (int mt = 0; mt < 2; ++mt) acc[mt][nt] = f32x4{0.f, 0.f, 0.f, 0.f};
  }
  f32x2* st_lds = (f32x2*)(lds + C::X_ST);

    {
      if constexpr (C::PST) {
        float s_ = (__uint_as_float(ps[0][0]) + __uint_as_float(ps[0][2])) + (__uint_as_float(ps[1][0]) + __uint_as_float(ps[1][2]));
        float q_ = (__uint_as_float(ps[0][1]) + __uint_as_float(ps[0][3])) + (__uint_as_float(ps[1][1]) + __uint_as_float(ps[1][3]));
        s_ = dpp_add<0xB1>(s_); q_ = dpp_add<0xB1>(q_);
        s_ = dpp_add<0x4E>(s_); q_ = dpp_add<0x4E>(q_);
        s_ = dpp_add<0x141>(s_); q_ = dpp_add<0x141>(q_);
        float mb, sd, m, v;
        ln_consts(s_, q_, mb, sd, m, v);
        float M = mb, R = 1.0f / sd;
        if constexpr (MODE == 4) {
          float m2 = (m - mb) * R, v2 = v * R * R;
          float m2b = rbf(m2);
          float sd2 = rbf(__builtin_sqrtf(rbf(rbf(v2) + 1e-5f)));
          M = mb + m2b * sd;
          R = R / sd2;
        }
        if ((tid & 7) == 0) st_lds[tid >> 3] = f32x2{M, R};
      }
    }
  f32x4 sx[2] = {f32x4{0.f, 0.f, 0.f, 0.f}, f32x4{0.f, 0.f, 0.f, 0.f}}, qx[2] = {sx[0], sx[0]};
  static_for<0, C::NB>([&](auto b_) {
    constexpr int s = b_.value;
    lds_bar();
    constexpr int S0 = s * C::SPB;
    constexpr int NSUB = ((S0 + C::SPB > C::NSTAGE) ? C::NSTAGE : S0 + C::SPB) - S0;
    constexpr int NKS = 2 * NSUB - ((MODE == 7 && S0 + NSUB == C::NSTAGE) ? 1 : 0);
    bf16x8 xf[NKS][2], wf[NKS][NT];
#pragma unroll
    for (int ks = 0; ks < NKS; ++ks) {
      const lds_cptr bs = (lds_cptr)lds + ((S0 + (ks >> 1)) % C::NSLOT) * C::SLOT;
      const int kk = ks & 1;
#pragma unroll
      for (int nt = 0; nt < NT; ++nt) {
        s16x4 w0 = lds_tr(bs + woff[kk][0] + nt * 128);
        s16x4 w1 = lds_tr(bs + woff[kk][1] + nt * 128);
        wf[ks][nt] = __builtin_shufflevector(w0, w1, 0, 1, 2, 3, 4, 5, 6, 7);
        if (nt == 0) {
#pragma unroll
          for (int mt = 0; mt < 2; ++mt) xf[ks][mt] = lds_rd128(bs + xoff[mt][kk]);
        }
      }
    }
#pragma unroll
    for (int ks = 0; ks < NKS; ++ks) {
#pragma unroll
      for (int nt = 0; nt < NT; ++nt) {
#pragma unroll
        for (int mt = 0; mt < 2; ++mt) {
          if (MODE == 1 && nt == 2) acc[mt][nt] = MFMA16(xf[ks][mt], wf[ks][nt], acc[mt][nt]);
          else acc[mt][nt] = MFMA16(wf[ks][nt], xf[ks][mt], acc[mt][nt]);
        }
      }
    }
    if constexpr (C::LN) {
      auto colsum = [&](auto par_) {
#pragma unroll
        for (int ks = par_.value; ks < NKS; ks += 2)
#pragma unroll
          for (int nt = 0; nt < NT; ++nt) {
            if (MODE == 1 && nt == 2) cs[nt] = MFMA16(ones8, wf[ks][nt], cs[nt]);
            else cs[nt] = MFMA16(wf[ks][nt], ones8, cs[nt]);
          }
      };
      if (wm == 0) colsum(std::integral_constant<int, 0>{});
      else colsum(std::integral_constant<int, 1>{});
    }
    if constexpr (MODE == 1) {
      auto stat = [&](const bf16x8 (&xx)[2]) {
#pragma unroll
        for (int mt = 0; mt < 2; ++mt) {
          sx[mt] = MFMA16(ones8, xx[mt], sx[mt]);
          qx[mt] = MFMA16(xx[mt], xx[mt], qx[mt]);
        }
      };
      if (wn == ((2 * s) & 3)) stat(xf[0]);
      else if (wn == ((2 * s + 1) & 3)) stat(xf[1]);
    }
    __builtin_amdgcn_sched_barrier(0);
  });

  float* cs_lds = (float*)(lds + C::X_CS);
  if constexpr (C::LN) {
#pragma unroll
    for (int nt = 0; nt < NT; ++nt) {
      float* d = cs_lds + ((wm * 4 + wn) * NT + nt) * 16;
      if (MODE == 1 && nt == 2) { if (g == 0) d[l15] = cs[nt][0]; }
      else if (l15 == 0) *(f32x4*)(d + 4 * g) = cs[nt];
    }
    if constexpr (MODE == 1) {
      f32x2* px = (f32x2*)(lds + C::X_PX);
      const int di = l15 & 3;
      float b0 = di == 0 ? qx[0][0] : di == 1 ? qx[0][1] : di == 2 ? qx[0][2] : qx[0][3];
      float b1 = di == 0 ? qx[1][0] : di == 1 ? qx[1][1] : di == 2 ? qx[1][2] : qx[1][3];
      if (g == (l15 >> 2)) { px[wn * 64 + wm * 32 + l15] = f32x2{sx[0][0], b0}; px[wn * 64 + wm * 32 + 16 + l15] = f32x2{sx[1][0], b1}; }
      lds_bar();
      if (tid < 64) {
        f32x2 t = (px[tid] + px[64 + tid]) + (px[128 + tid] + px[192 + tid]);
        float mb, sd, m, v;
        ln_consts(t[0], t[1], mb, sd, m, v);
        st_lds[tid] = f32x2{mb, 1.0f / sd};
      }
    }
    lds_bar();
#pragma unroll
    for (int nt = 0; nt < NT; ++nt) {
      const float* d = cs_lds + (((1 - wm) * 4 + wn) * NT + nt) * 16;
      if (MODE == 1 && nt == 2) cs[nt][0] += d[l15];
      else cs[nt] += *(const f32x4*)(d + 4 * g);
    }
  }
  char* ost = lds + C::X_OUT;
  float kn2 = 0.f;
#pragma unroll
  for (int mt = 0; mt < 2; ++mt) {
    const int lrow = wm * 32 + mt * 16 + l15;
    const int tok = row0 + lrow;
#pragma unroll
    for (int nt = 0; nt < NT; ++nt) {
      f32x4 a = acc[mt][nt];
      if constexpr (C::LN) {
        if (MODE == 1 && nt == 2) {
          float c = cs[nt][0];
#pragma unroll
          for (int i = 0; i < 4; ++i) {
            f32x2 st = st_lds[wm * 32 + mt * 16 + 4 * g + i];
            a[i] = (a[i] - st[0] * c) * st[1];
          }
        } else {
          f32x4 c = cs[nt];
          f32x2 st = st_lds[lrow];
#pragma unroll
          for (int i = 0; i < 4; ++i) a[i] = (a[i] - st[0] * c[i]) * st[1];
        }
      }
      u32x2 v;
      if constexpr (MODE == 1) {
        const int head = cb * 4 + wn;
        if (nt < 2) {
          float sc = (nt == 0) ? 0.25f * 1.4426950408889634f : 1.0f;
          v = u32x2{cvtpk(a[0] * sc, a[1] * sc), cvtpk(a[2] * sc, a[3] * sc)};
          u16* dst = (nt == 0 ? ga.o0 : ga.o1) + ((size_t)(head * 64 + (tok >> 5)) * 2 + (g >> 1)) * 256 + (tok & 31) * 8 + (g & 1) * 4;
          st_wt2(dst, v);
          if (nt == 1) {
            float n2 = dot2u(v[0], v[0], dot2u(v[1], v[1], 0.f));
            n2 = row16_max(add_x32(add_x16(n2)));
            kn2 = fmaxf(kn2, n2);
            if (mt == 1 && lane == 0) ga.pout[head * 64 + rb * 2 + wm] = kn2;
          }
        } else {
          const int step = (row0 + wm * 32 + mt * 16) >> 4;
          v = u32x2{cvtpk(a[0], a[1]), cvtpk(a[2], a[3])};
          u16* dst = ga.o2 + ((size_t)(head * 128 + step) * 2 + (g & 1)) * 128 + l15 * 8 + (g >> 1) * 4;
          st_wt2(dst, v);
        }
      } else if constexpr (MODE == 3 || MODE == 5) {
        u32x2 r = res0[mt];
        if constexpr (MODE == 5) {
          f32x2 st = st_lds[lrow];
          unsigned d0 = cvtpk(lo2f(r[0]) - st[0], hi2f(r[0]) - st[0]), d1 = cvtpk(lo2f(r[1]) - st[0], hi2f(r[1]) - st[0]);
          r[0] = cvtpk(lo2f(d0) * st[1], hi2f(d0) * st[1]);
          r[1] = cvtpk(lo2f(d1) * st[1], hi2f(d1) * st[1]);
        }
        unsigned a0 = cvtpk(a[0], a[1]), a1 = cvtpk(a[2], a[3]);
        v = u32x2{cvtpk(lo2f(r[0]) + lo2f(a0), hi2f(r[0]) + hi2f(a0)), cvtpk(lo2f(r[1]) + lo2f(a1), hi2f(r[1]) + hi2f(a1))};
        float f0 = lo2f(v[0]), f1 = hi2f(v[0]), f2 = lo2f(v[1]), f3 = hi2f(v[1]);
        float s_ = (f0 + f1) + (f2 + f3), q_ = fmaf(f0, f0, f1 * f1) + fmaf(f2, f2, f3 * f3);
        s_ = add_x32(add_x16(s_)); q_ = add_x32(add_x16(q_));
        if (g == 0) *(f32x2*)(ga.pout + (size_t)tok * 64 + (cb * 4 + wn) * 2) = f32x2{s_, q_};
      } else if constexpr (MODE == 4 || MODE == 6) {
        unsigned a0 = cvtpk(a[0], a[1]), a1 = cvtpk(a[2], a[3]);
        v = u32x2{cvtpk(gelu_tanh(lo2f(a0)), gelu_tanh(hi2f(a0))), cvtpk(gelu_tanh(lo2f(a1)), gelu_tanh(hi2f(a1)))};
      } else {
        u32x2 x1 = res0[mt], hh = res1[mt];
        unsigned a0 = cvtpk(a[0], a[1]), a1 = cvtpk(a[2], a[3]);
        unsigned b0 = cvtpk(lo2f(x1[0]) + lo2f(a0), hi2f(x1[0]) + hi2f(a0));
        unsigned b1 = cvtpk(lo2f(x1[1]) + lo2f(a1), hi2f(x1[1]) + hi2f(a1));
        v = u32x2{cvtpk(lo2f(hh[0]) + lo2f(b0), hi2f(hh[0]) + hi2f(b0)), cvtpk(lo2f(hh[1]) + lo2f(b1), hi2f(hh[1]) + hi2f(b1))};
      }
      if constexpr (MODE != 1) *(u32x2*)(ost + lrow * C::RSO + nt * 128 + wn * 32 + g * 8) = v;
    }
  }
  if constexpr (MODE != 1) {
    lds_bar();
#pragma unroll
    for (int j = 0; j < NT; ++j) {
      const int c = tid + 512 * j, row = c / (8 * NT), cc = c % (8 * NT);
      const u32x4 v = *(const u32x4*)(ost + row * C::RSO + cc * 16);
      const int tok = row0 + row;
      if constexpr (MODE == 4) {
        const int grp = cb >> 1;
        const int sh = (grp == 0) ? 1 : (grp == 1) ? -1 : (grp == 2) ? 2 : -2;
        const int col = cb * 64 + cc * 8, dt = tok + sh, src = tok - sh;
        if (dt >= 0 && dt < SEQ) st_wt4(ga.o0 + (size_t)dt * DM + col, v);
        if (src < 0 || src >= SEQ) *(u32x4*)(ga.o0 + (size_t)tok * DM + col) = u32x4{0u, 0u, 0u, 0u};
      } else if constexpr (MODE == 6) {
        const int col = cb * 192 + cc * 8;
        if (col < FF) st_wt4(ga.o0 + (size_t)tok * FF + col, v);
      } else {
        st_wt4(ga.o0 + (size_t)tok * DM + cb * 64 + cc * 8, v);
      }
    }
  }
}

constexpr int AT_K = 0, AT_V = 65536, AT_ONES = 131072, AT_Q = AT_ONES + 8192, AT_LDS = AT_Q + 8192;

__global__ __launch_bounds__(640, 2) void attn_kernel(const u16* Qf, const u16* Kf, const u16* Vf, u16* O, const float* KN,
                                                       const u16* wa, const u16* wb, const u16* wc, const u16* wd, const u16* we) {
  extern __shared__ __attribute__((aligned(16))) char lds[];
  const int tid = threadIdx.x, lane = tid & 63;
  const int wid = __builtin_amdgcn_readfirstlane(tid >> 6);
  const int xcd = blockIdx.x & 7, idx = blockIdx.x >> 3;
  const int head = xcd * 4 + (idx >> 3), qb = idx & 7;
  const unsigned lds0 = (unsigned)(uintptr_t)lds;
  const unsigned lo = lane * 16;

  if (wid >= 8) {
    const int L = wid - 8;
    const char* src = (const char*)(L ? Vf : Kf) + (size_t)head * 65536;
    const char* qg = (const char*)Qf + (size_t)head * 65536 + (qb * 8 + L * 4) * 1024;
    const unsigned dst = lds0 + (L ? AT_V : AT_K);
#pragma unroll
    for (int j = 0; j < 4; ++j) glds16(qg, lo + j * 1024, lds0 + AT_Q + (L * 4 + j) * 1024);
#pragma unroll
    for (int j = 0; j < 16; ++j) glds16(src, lo + j * 1024, dst + j * 1024);
    wait_vm_bar<0>();
#pragma unroll
    for (int j = 16; j < 64; ++j) glds16(src, lo + j * 1024, dst + j * 1024);
    unsigned tw[5][3];
#pragma unroll
    for (int j = 0; j < 3; ++j) {
      unsigned t = (unsigned)((idx * 2 + L) * 192 + j * 64 + lane) * 128u;
      unsigned ta = t < 512u * 1024u ? t : 0u, tb = t < (unsigned)(DM * FF * 2) ? t : 0u;
      asm volatile("global_load_dword %0, %5, %7\n\tglobal_load_dword %1, %5, %8\n\tglobal_load_dword %2, %5, %9\n\t"
                   "global_load_dword %3, %6, %10\n\tglobal_load_dword %4, %6, %11"
                   : "=&v"(tw[0][j]), "=&v"(tw[1][j]), "=&v"(tw[2][j]), "=&v"(tw[3][j]), "=&v"(tw[4][j])
                   : "v"(ta), "v"(tb), "s"(wa), "s"(wb), "s"(wc), "s"(wd), "s"(we)
                   : "memory");
    }
    asm volatile("s_waitcnt vmcnt(15)\n\ts_barrier\n\ts_waitcnt vmcnt(0)"
                 : "+v"(tw[0][0]), "+v"(tw[1][0]), "+v"(tw[2][0]), "+v"(tw[3][0]), "+v"(tw[4][0]), "+v"(tw[0][1]), "+v"(tw[1][1]),
                   "+v"(tw[2][1]), "+v"(tw[3][1]), "+v"(tw[4][1]), "+v"(tw[0][2]), "+v"(tw[1][2]), "+v"(tw[2][2]), "+v"(tw[3][2]),
                   "+v"(tw[4][2])::"memory");
    return;
  }
  const int r = lane & 31, h = lane >> 5;
  const int qt = wid;
  *(u32x4*)(lds + AT_ONES + tid * 16) = u32x4{0x3F803F80u, 0x3F803F80u, 0x3F803F80u, 0x3F803F80u};

  const float kn_l = KN[head * 64 + lane];
  const lds_cptr kp0 = (lds_cptr)lds + AT_K + lane * 16;
  const lds_cptr vp0_ = (lds_cptr)lds + ((r < 16) ? (AT_V + h * 256 + r * 16) : (AT_ONES + (r - 16) * 16));
  const int vstep = (r < 16) ? 4096 : 0;
  lds_cptr kp, vp;
  f32x16 o, negm, sA, sB;
  bf16x8 kfA, kfB, vA0, vA1, vB0, vB1;
  u32x4 pA0, pA1, pB0, pB1;

  lds_bar();
  const bf16x8 qf = lds_rd128((lds_cptr)lds + AT_Q + qt * 1024 + lane * 16);
  float rbound;
  {
    const u32x4 qu = __builtin_bit_cast(u32x4, qf);
    float q2 = dot2u(qu[0], qu[0], dot2u(qu[1], qu[1], dot2u(qu[2], qu[2], dot2u(qu[3], qu[3], 0.f))));
    q2 = add_x32(q2);
    const float k2 = max_x32(max_x16(row16_max(kn_l)));
    rbound = __builtin_sqrtf(q2 * k2) * 1.001f + 0.01f;
  }

#define SBAR() __builtin_amdgcn_sched_barrier(0)
#define PK(x, i) __builtin_amdgcn_perm(__float_as_uint(x[(i) + 1]), __float_as_uint(x[i]), 0x07060302u)
#define EXPS(lo_, hi_) _Pragma("unroll") for (int i = lo_; i < hi_; ++i) sc[i] = __builtin_amdgcn_exp2f(sc[i])
  auto pass = [&](auto safe_) {
    constexpr bool SAFE = safe_.value;
    kp = kp0; vp = vp0_;
#pragma unroll
    for (int i = 0; i < 16; ++i) { o[i] = 0.f; negm[i] = SAFE ? 0.f : -rbound; }
    sA = MFMA32(lds_rd128(kp), qf, negm);
    kfA = lds_rd128(kp + 1024);
    if constexpr (SAFE) {
      float mx = sA[0];
#pragma unroll
      for (int i = 1; i < 16; ++i) mx = fmaxf(mx, sA[i]);
      const float d = max_x32(mx) + 7.0f;
#pragma unroll
      for (int i = 0; i < 16; ++i) { negm[i] = -d; sA[i] -= d; }
    }
    vB0 = lds_rd128(vp); vB1 = vB0;
    pB0 = u32x4{0u, 0u, 0u, 0u}; pB1 = pB0;
    auto step = [&](f32x16& sc, f32x16& sn, const bf16x8& kf, bf16x8& kfn, const bf16x8& vq0, const bf16x8& vq1,
                    const u32x4& pp0, const u32x4& pp1, bf16x8& vc0, bf16x8& vc1, u32x4& pc0, u32x4& pc1, lds_cptr kc,
                    lds_cptr vc) {
      sn = MFMA32(kf, qf, negm);
      kfn = lds_rd128(kc + 2048);
      vc0 = lds_rd128(vc); vc1 = lds_rd128(vc + 512);
      SBAR();
      EXPS(0, 5);
      SBAR();
      o = MFMA32(vq0, __builtin_bit_cast(bf16x8, pp0), o);
      SBAR();
      EXPS(5, 11);
      SBAR();
      o = MFMA32(vq1, __builtin_bit_cast(bf16x8, pp1), o);
      SBAR();
      EXPS(11, 16);
      SBAR();
#pragma unroll
      for (int i = 0; i < 4; ++i) { pc0[i] = PK(sc, 2 * i); pc1[i] = PK(sc, 8 + 2 * i); }
      if constexpr (SAFE) {
        unsigned chk = (pc0[0] | pc0[1] | pc0[2]) | (pc0[3] | pc1[0] | pc1[1]) | (pc1[2] | pc1[3]);
        if (__builtin_expect(__any((chk & 0x40004000u) != 0), 0)) {
          f32x16 s2 = MFMA32(lds_rd128(kc), qf, negm);
          float mx = s2[0];
#pragma unroll
          for (int i = 1; i < 16; ++i) mx = fmaxf(mx, s2[i]);
          const float d = fmaxf(max_x32(mx) + 7.0f, 0.f);
          const float alpha = __builtin_amdgcn_exp2f(-d);
#pragma unroll
          for (int i = 0; i < 16; ++i) { o[i] *= alpha; negm[i] -= d; sn[i] -= d; s2[i] = __builtin_amdgcn_exp2f(s2[i] - d); }
#pragma unroll
          for (int i = 0; i < 4; ++i) { pc0[i] = PK(s2, 2 * i); pc1[i] = PK(s2, 8 + 2 * i); }
        }
      }
      SBAR();
    };
    auto group = [&]() {
      step(sA, sB, kfA, kfB, vB0, vB1, pB0, pB1, vA0, vA1, pA0, pA1, kp, vp);
      step(sB, sA, kfB, kfA, vA0, vA1, pA0, pA1, vB0, vB1, pB0, pB1, kp + 1024, vp + 1024);
      step(sA, sB, kfA, kfB, vB0, vB1, pB0, pB1, vA0, vA1, pA0, pA1, kp + 2048, vp + 2048);
      step(sB, sA, kfB, kfA, vA0, vA1, pA0, pA1, vB0, vB1, pB0, pB1, kp + 3072, vp + 3072);
      kp += 4096;
      vp += vstep;
    };
    if constexpr (!SAFE) {
#pragma unroll 1
      for (int it = 0; it < 3; ++it) group();
      lds_bar();
#pragma unroll 1
      for (int it = 3; it < 16; ++it) group();
    } else {
#pragma unroll 1
      for (int it = 0; it < 16; ++it) group();
    }
    o = MFMA32(vB0, __builtin_bit_cast(bf16x8, pB0), o);
    o = MFMA32(vB1, __builtin_bit_cast(bf16x8, pB1), o);
  };
  pass(std::false_type{});
  if (__builtin_expect(__any(!(o[8] >= 8.0779357e-28f && o[8] <= 4096.f)), 0)) pass(std::true_type{});
  {
    const float inv = 1.0f / o[8];
    const int q = qb * 256 + qt * 32 + r;
    u16* dst = O + (size_t)q * DM + head * 16 + 4 * h;
    *(u32x2*)dst = u32x2{cvtpk(o[0] * inv, o[1] * inv), cvtpk(o[2] * inv, o[3] * inv)};
    *(u32x2*)(dst + 8) = u32x2{cvtpk(o[4] * inv, o[5] * inv), cvtpk(o[6] * inv, o[7] * inv)};
  }
}

template <int MODE>
static void launch_gemm(const GArgs& ga, hipStream_t stream) {
  (void)hipFuncSetAttribute((const void*)gemm_kernel<MODE>, hipFuncAttributeMaxDynamicSharedMemorySize, Cfg<MODE>::LDS);
  gemm_kernel<MODE><<<256, 512 + 64 * NLD, Cfg<MODE>::LDS, stream>>>(ga);
}

extern "C" void kernel_launch(void* const* d_in, const int* in_sizes, int n_in, void* d_out, int out_size, void* d_ws,
                              size_t ws_size, hipStream_t stream) {
  const u16* x = (const u16*)d_in[0];
  const u16* wqkv = (const u16*)d_in[1];
  const u16* wo = (const u16*)d_in[2];
  const u16* w1 = (const u16*)d_in[4];
  const u16* w2 = (const u16*)d_in[5];
  const u16* w3 = (const u16*)d_in[6];
  const u16* w4 = (const u16*)d_in[7];
  char* ws = (char*)d_ws;
  u16 *Q = (u16*)(ws + WS_Q), *Kf = (u16*)(ws + WS_K), *Vf = (u16*)(ws + WS_V), *O = (u16*)(ws + WS_O),
      *H = (u16*)(ws + WS_H), *T = (u16*)(ws + WS_T), *X1 = (u16*)(ws + WS_X1), *U = (u16*)(ws + WS_U);
  float *PH = (float*)(ws + WS_PH), *PX = (float*)(ws + WS_PX), *KN = (float*)(ws + WS_KN);
  u16* out = (u16*)d_out;

  launch_gemm<1>(GArgs{x, wqkv, nullptr, nullptr, nullptr, KN, Q, Kf, Vf}, stream);
  (void)hipFuncSetAttribute((const void*)attn_kernel, hipFuncAttributeMaxDynamicSharedMemorySize, AT_LDS);
  attn_kernel<<<256, 640, AT_LDS, stream>>>(Q, Kf, Vf, O, KN, wo, w1, w2, w3, w4);
  launch_gemm<3>(GArgs{O, wo, x, nullptr, nullptr, PH, H, nullptr, nullptr}, stream);
  launch_gemm<4>(GArgs{H, w1, nullptr, nullptr, PH, nullptr, T, nullptr, nullptr}, stream);
  launch_gemm<5>(GArgs{T, w2, H, nullptr, PH, PX, X1, nullptr, nullptr}, stream);
  launch_gemm<6>(GArgs{X1, w3, nullptr, nullptr, PX, nullptr, U, nullptr, nullptr}, stream);
  launch_gemm<7>(GArgs{U, w4, X1, H, nullptr, nullptr, out, nullptr, nullptr}, stream);
}
```
